```python
import torch

def _softmax(x, axis=-1):
    return torch.softmax(x, dim=axis)
def _gelu(x):
    return torch.nn.functional.gelu(x, approximate="tanh")
def _silu(x):
    return torch.nn.functional.silu(x)
def _sigmoid(x):
    return torch.sigmoid(x)
def _layernorm(x, axis=-1, eps=1e-5):
    m = x.mean(dim=axis, keepdim=True)
    v = x.var(dim=axis, keepdim=True, unbiased=False)
    return (x - m) / torch.sqrt(v + eps)
def _scatter_add(tgt, idx, src, axis=0):
    return tgt.clone().index_add_(axis, idx, src)
def _segment_sum(src, seg, n):
    out = torch.zeros((n,) + src.shape[1:], dtype=src.dtype)
    return out.index_add_(0, seg, src)
def _pad(x, pw):
    flat = tuple(v for pair in reversed(pw) for v in pair)
    return torch.nn.functional.pad(x, flat)

kernel_name = 'concat_heads__compressed_kv_cla__compressed_kv_cla__decode'

def setup_inputs():

    _w = (lambda a: (lambda t: t.to(torch.bfloat16) if t.is_floating_point() else t)(torch.as_tensor(a)))
    def _A_setup():
        B, H, Sq, Sk, D, M = 4, 32, 1, 4096, 128, 512
        Dc = 4 * D
        return {
            "x": _w((lambda *s: torch.randn(*s, dtype=torch.bfloat16) * 3.2)(B, Sq, M)),
            "c_kv": _w((lambda *s: torch.randn(*s, dtype=torch.bfloat16) * 0.05)(B, Sk, Dc)),
            "w_q": _w((lambda *s: torch.randn(*s, dtype=torch.bfloat16) * 3.2)(M, H, D) * (M ** -0.5)),
            "w_uk": _w((lambda *s: torch.randn(*s, dtype=torch.bfloat16) * 3.2)(Dc, H, D) * (Dc ** -0.5)),
            "w_uv": _w((lambda *s: torch.randn(*s, dtype=torch.bfloat16) * 0.05)(Dc, H, D) * (Dc ** -0.5)),
            "w_o": _w((lambda *s: torch.randn(*s, dtype=torch.bfloat16) * 0.05)(H, D, M) * ((H * D) ** -0.5)),
        }
    _A_in = _A_setup()

    def _B_setup():
        B, H, Sq, Sk, D, M = 4, 32, 1, 4096, 128, 512
        Dc = 4 * D
        return {
            "x": _w((lambda *s: torch.randn(*s, dtype=torch.bfloat16) * 3.2)(B, Sq, M)),
            "c_kv": _w((lambda *s: torch.randn(*s, dtype=torch.bfloat16) * 0.05)(B, Sk, Dc)),
            "w_q": _w((lambda *s: torch.randn(*s, dtype=torch.bfloat16) * 3.2)(M, H, D) * (M ** -0.5)),
            "w_uk": _w((lambda *s: torch.randn(*s, dtype=torch.bfloat16) * 3.2)(Dc, H, D) * (Dc ** -0.5)),
            "w_uv": _w((lambda *s: torch.randn(*s, dtype=torch.bfloat16) * 0.05)(Dc, H, D) * (Dc ** -0.5)),
            "w_o": _w((lambda *s: torch.randn(*s, dtype=torch.bfloat16) * 0.05)(H, D, M) * ((H * D) ** -0.5)),
        }
    _B_in = _B_setup()

    return {
        "A_x": _A_in["x"],
        "A_c_kv": _A_in["c_kv"],
        "A_w_q": _A_in["w_q"],
        "A_w_uk": _A_in["w_uk"],
        "A_w_uv": _A_in["w_uv"],
        "A_w_o": _A_in["w_o"],
        "B_x": _B_in["x"],
        "B_c_kv": _B_in["c_kv"],
        "B_w_q": _B_in["w_q"],
        "B_w_uk": _B_in["w_uk"],
        "B_w_uv": _B_in["w_uv"],
        "B_w_o": _B_in["w_o"],
    }

def reference(A_x, A_c_kv, A_w_q, A_w_uk, A_w_uv, A_w_o, B_x, B_c_kv, B_w_q, B_w_uk, B_w_uv, B_w_o):
    def _A_ref(x, c_kv, w_q, w_uk, w_uv, w_o):
        B, Sq, M = x.shape[0], x.shape[1], x.shape[2]
        Sk = c_kv.shape[1]
        H, D = w_q.shape[1], w_q.shape[2]
        q = torch.einsum("bqm,mhd->bhqd", x, w_q)
        k = torch.einsum("bkc,chd->bhkd", c_kv, w_uk)
        v = torch.einsum("bkc,chd->bhkd", c_kv, w_uv)
        scores = torch.einsum("bhqd,bhkd->bhqk", q, k) * (D ** -0.5)
        off = Sk - Sq
        qi = torch.arange(Sq)[:, None]
        kj = torch.arange(Sk)[None, :]
        cmask = kj <= (qi + off)
        scores = torch.where(cmask[None, None], scores, -1e9)
        p = _softmax(scores, axis=-1)
        ctx = torch.einsum("bhqk,bhkd->bhqd", p, v)
        return torch.einsum("bhqd,hdm->bqm", ctx, w_o)

    def _B_ref(x, c_kv, w_q, w_uk, w_uv, w_o):
        B, Sq, M = x.shape[0], x.shape[1], x.shape[2]
        Sk = c_kv.shape[1]
        H, D = w_q.shape[1], w_q.shape[2]
        q = torch.einsum("bqm,mhd->bhqd", x, w_q)
        k = torch.einsum("bkc,chd->bhkd", c_kv, w_uk)
        v = torch.einsum("bkc,chd->bhkd", c_kv, w_uv)
        scores = torch.einsum("bhqd,bhkd->bhqk", q, k) * (D ** -0.5)
        off = Sk - Sq
        qi = torch.arange(Sq)[:, None]
        kj = torch.arange(Sk)[None, :]
        cmask = kj <= (qi + off)
        scores = torch.where(cmask[None, None], scores, -1e9)
        p = _softmax(scores, axis=-1)
        ctx = torch.einsum("bhqk,bhkd->bhqd", p, v)
        return torch.einsum("bhqd,hdm->bqm", ctx, w_o)

    a = _A_ref(A_x, A_c_kv, A_w_q, A_w_uk, A_w_uv, A_w_o)
    b = _B_ref(B_x, B_c_kv, B_w_q, B_w_uk, B_w_uv, B_w_o)
    return torch.concatenate([a, b], axis=-1)
```

```cpp
#include <hip/hip_runtime.h>
#include <hip/hip_bf16.h>
typedef unsigned short u16;
__device__ __forceinline__ float bf2f(u16 b){ return __uint_as_float(((unsigned)b)<<16); }
__device__ __forceinline__ u16 f2bf(float f){ unsigned u=__float_as_uint(f); u += 0x7fffu + ((u>>16)&1u); return (u16)(u>>16); }
constexpr int B=4,H=32,SK=4096,D=128,M=512,DC=512;
struct P { const u16* x[2]; const u16* ckv[2]; const u16* wq[2]; const u16* wuk[2]; const u16* wuv[2]; const u16* wo[2]; };

__global__ void k1(P pp, float* qabs){
  int p=blockIdx.x>>5, h=blockIdx.x&31, t=threadIdx.x;
  __shared__ float q[B][D];
  { int b=t>>7, d=t&127; float s=0; for(int m=0;m<M;m++) s+=bf2f(pp.x[p][b*M+m])*bf2f(pp.wq[p][(m*H+h)*D+d]); q[b][d]=bf2f(f2bf(s)); }
  __syncthreads();
  int c=t; float s[B]={0,0,0,0};
  for(int d=0;d<D;d++){ float w=bf2f(pp.wuk[p][(c*H+h)*D+d]); for(int b=0;b<B;b++) s[b]+=q[b][d]*w; }
  for(int b=0;b<B;b++) qabs[((p*B+b)*H+h)*DC+c]=s[b]*0.08838834764831845f;
}
__global__ void k2(P pp, const float* qabs, float* lat){
  int pb=blockIdx.x>>5, h=blockIdx.x&31, t=threadIdx.x; int p=pb>>2,b=pb&3;
  __shared__ float qa[DC]; __shared__ float sc[SK]; __shared__ float red[256];
  for(int c=t;c<DC;c+=256) qa[c]=qabs[(pb*H+h)*DC+c];
  __syncthreads();
  const u16* kv=pp.ckv[p]+(size_t)b*SK*DC;
  float mx=-1e30f;
  for(int k=t;k<SK;k+=256){ float s=0; for(int c=0;c<DC;c++) s+=qa[c]*bf2f(kv[(size_t)k*DC+c]); sc[k]=s; mx=fmaxf(mx,s);}
  red[t]=mx; __syncthreads(); for(int o=128;o>0;o>>=1){ if(t<o) red[t]=fmaxf(red[t],red[t+o]); __syncthreads(); } mx=red[0]; __syncthreads();
  float sum=0; for(int k=t;k<SK;k+=256){ float e=__expf(sc[k]-mx); sc[k]=e; sum+=e; }
  red[t]=sum; __syncthreads(); for(int o=128;o>0;o>>=1){ if(t<o) red[t]+=red[t+o]; __syncthreads(); } sum=red[0]; __syncthreads();
  for(int c=t;c<DC;c+=256){ float s=0; for(int k=0;k<SK;k++) s+=sc[k]*bf2f(kv[(size_t)k*DC+c]); lat[(pb*H+h)*DC+c]=s/sum; }
}
__global__ void k3(P pp, const float* lat, float* ctx){
  int pb=blockIdx.x>>5, h=blockIdx.x&31, d=threadIdx.x; int p=pb>>2;
  float s=0; for(int c=0;c<DC;c++) s+=lat[(pb*H+h)*DC+c]*bf2f(pp.wuv[p][(c*H+h)*D+d]);
  ctx[(pb*H+h)*D+d]=s;
}
__global__ void k4(P pp, const float* ctx, u16* out){
  int pb=blockIdx.x, m=threadIdx.x; int p=pb>>2,b=pb&3;
  float s=0; for(int i=0;i<H*D;i++) s+=ctx[pb*H*D+i]*bf2f(pp.wo[p][(size_t)i*M+m]);
  out[b*1024+p*512+m]=f2bf(s);
}
extern "C" void kernel_launch(void* const* d_in, const int* in_sizes, int n_in, void* d_out, int out_size, void* d_ws, size_t ws_size, hipStream_t stream){
  P pp; for(int p=0;p<2;p++){ pp.x[p]=(const u16*)d_in[6*p+0]; pp.ckv[p]=(const u16*)d_in[6*p+1]; pp.wq[p]=(const u16*)d_in[6*p+2]; pp.wuk[p]=(const u16*)d_in[6*p+3]; pp.wuv[p]=(const u16*)d_in[6*p+4]; pp.wo[p]=(const u16*)d_in[6*p+5]; }
  float* qabs=(float*)d_ws; float* lat=qabs+8*H*DC; float* ctx=lat+8*H*DC;
  k1<<<64,512,0,stream>>>(pp,qabs);
  k2<<<256,256,0,stream>>>(pp,qabs,lat);
  k3<<<256,128,0,stream>>>(pp,lat,ctx);
  k4<<<8,512,0,stream>>>(pp,ctx,(u16*)d_out);
}
```

```cpp
#include <hip/hip_runtime.h>
#include <hip/hip_bf16.h>

#define GAS __attribute__((address_space(1)))
#define LAS __attribute__((address_space(3)))
typedef unsigned v4u __attribute__((ext_vector_type(4)));
typedef unsigned v2u __attribute__((ext_vector_type(2)));
typedef float f32x4 __attribute__((ext_vector_type(4)));
typedef float f32x16 __attribute__((ext_vector_type(16)));
typedef short bf16x8 __attribute__((ext_vector_type(8)));
typedef short s16x4 __attribute__((ext_vector_type(4)));
typedef GAS unsigned gu32;
#define RLX_AGENT __ATOMIC_RELAXED, __HIP_MEMORY_SCOPE_AGENT
#define RLX_WG __ATOMIC_RELAXED, __HIP_MEMORY_SCOPE_WORKGROUP
#define DI __device__ __forceinline__

#define NK 1
#define KV_NT 1
#define STAMPS 0

constexpr unsigned WS_F1 = 0;
constexpr unsigned WS_F1_BYTES = 8 * 128 * 4;
constexpr unsigned WS_QG = 8192;
constexpr unsigned WS_F2 = WS_QG + 8 * 32768;
constexpr unsigned WS_OG = WS_F2 + 256 * 1024;
constexpr unsigned WS_PO = WS_OG + 256 * 32768;
constexpr unsigned WS_ST = WS_PO + 256 * 8192;
constexpr unsigned WS_END = WS_ST + 256 * 8 * 32 * 8;

constexpr unsigned L_T = 0;
constexpr unsigned L_QP = 131072;
constexpr unsigned L_QA = L_QP + 16 * 272;
constexpr unsigned L_P = L_QA + 1024;
constexpr unsigned L_MX = L_P + 8192;
constexpr unsigned L_LS = L_MX + 512;
constexpr unsigned L_CT = L_LS + 512;
constexpr unsigned L_END = L_CT + 128;
enum { C_QP = 0, C_QA, C_F1, C_KV0, C_KV1, C_KV2, C_KV3, C_ISS, C_MX, C_P, C_LAT, C_CTX, C_N };

constexpr unsigned SENT = 0xFFFFFFFFu;
constexpr unsigned SPIN_CAP = 1u << 16;
constexpr float QSCALE = 0.08838834764831845f * 1.4426950408889634f;

struct Params {
  const void* x[2]; const void* ckv[2]; const void* wq[2]; const void* wuk[2]; const void* wuv[2]; const void* wo[2];
  void* out; void* ws;
};

DI f32x4 mfma16(v4u a, v4u b, f32x4 c) {
  return __builtin_amdgcn_mfma_f32_16x16x32_bf16(__builtin_bit_cast(bf16x8, a), __builtin_bit_cast(bf16x8, b), c, 0, 0, 0);
}
DI f32x16 mfma32(v4u a, v4u b, f32x16 c) {
  return __builtin_amdgcn_mfma_f32_32x32x16_bf16(__builtin_bit_cast(bf16x8, a), __builtin_bit_cast(bf16x8, b), c, 0, 0, 0);
}
typedef __bf16 bf2_t __attribute__((ext_vector_type(2)));
typedef float f2_t __attribute__((ext_vector_type(2)));
DI unsigned pk(float lo, float hi) {
  f2_t f = {lo, hi};
  return __builtin_bit_cast(unsigned, __builtin_convertvector(f, bf2_t));
}
DI v4u tcol(const v4u (&w)[8], int i) {
  const unsigned sel = (i & 1) ? 0x07060302u : 0x05040100u;
  v4u o;
  o.x = __builtin_amdgcn_perm(w[1][i >> 1], w[0][i >> 1], sel);
  o.y = __builtin_amdgcn_perm(w[3][i >> 1], w[2][i >> 1], sel);
  o.z = __builtin_amdgcn_perm(w[5][i >> 1], w[4][i >> 1], sel);
  o.w = __builtin_amdgcn_perm(w[7][i >> 1], w[6][i >> 1], sel);
  return o;
}
#define CFENCE() asm volatile("" ::: "memory")
DI void lds_signal(LAS unsigned* c, int lane) {
  asm volatile("s_waitcnt lgkmcnt(0)" ::: "memory");
  if (lane == 0) __hip_atomic_fetch_add(c, 1u, RLX_WG);
  CFENCE();
}
DI void lds_wait(LAS unsigned* c, unsigned target) {
  CFENCE();
  while ((unsigned)__builtin_amdgcn_readfirstlane(__hip_atomic_load(c, RLX_WG)) < target) __builtin_amdgcn_s_sleep(1);
  CFENCE();
}
DI float xor32_max(float v) {
  auto r = __builtin_amdgcn_permlane32_swap(__float_as_uint(v), __float_as_uint(v), false, false);
  return fmaxf(__uint_as_float(r[0]), __uint_as_float(r[1]));
}
DI float xor32_sum(float v) {
  auto r = __builtin_amdgcn_permlane32_swap(__float_as_uint(v), __float_as_uint(v), false, false);
  return __uint_as_float(r[0]) + __uint_as_float(r[1]);
}
DI float xor16_max(float v) {
  auto r = __builtin_amdgcn_permlane16_swap(__float_as_uint(v), __float_as_uint(v), false, false);
  return fmaxf(__uint_as_float(r[0]), __uint_as_float(r[1]));
}
DI float xor16_sum(float v) {
  auto r = __builtin_amdgcn_permlane16_swap(__float_as_uint(v), __float_as_uint(v), false, false);
  return __uint_as_float(r[0]) + __uint_as_float(r[1]);
}
DI void dma16(const void* sbase, unsigned voff, unsigned lds_dst) {
  unsigned keep;
  asm volatile("s_mov_b32 %0, m0\n\ts_mov_b32 m0, %3\n\ts_nop 0\n\tglobal_load_lds_dwordx4 %1, %2 nt\n\ts_mov_b32 m0, %0"
               : "=&s"(keep) : "v"(voff), "s"(sbase), "s"(lds_dst) : "memory");
}
#define STAMP(i) do {} while (0)
#define RSRC(ptr, bytes) __builtin_amdgcn_make_buffer_rsrc((void*)(ptr), 0, (int)(bytes), 0x00020000)
#define BLD(rs, vo, so, aux) __builtin_amdgcn_raw_buffer_load_b128(rs, (int)(vo), (int)(so), aux)
constexpr int SC1 = 16, NT = 2;

__global__ void __launch_bounds__(512, 2) mla_fused(Params P) {
  extern __shared__ __attribute__((aligned(16))) unsigned char smem[];
  LAS unsigned char* L = (LAS unsigned char*)smem;
  LAS unsigned* ctl = (LAS unsigned*)(L + L_CT);
  const int tid = threadIdx.x, lane = tid & 63;
  const int wave = __builtin_amdgcn_readfirstlane(tid >> 6);
  const int w = blockIdx.x;
  const int p1 = (w >> 5) & 1, h = w & 31, cq = w >> 6;
  const int src = h * 4 + cq;
  const int blk1 = p1 * 128 + src;
  const int pb = w & 7, p2 = pb >> 2, b2 = pb & 3, j = w >> 3;
  const int r = lane & 15, g = lane >> 4;
  const __amdgpu_buffer_rsrc_t ws = RSRC(P.ws, WS_END);
  STAMP(0);
  if (tid < C_N) ctl[tid] = 0u;

  if (wave < 4) {
    const int aw = wave;
    {
      const v4u z = {0u, 0u, 0u, 0u}, s = {SENT, SENT, SENT, SENT};
      if (aw == 0) __builtin_amdgcn_raw_buffer_store_b128(z, ws, (int)(WS_F2 + blk1 * 1024 + lane * 16), 0, SC1);
      __builtin_amdgcn_raw_buffer_store_b128(s, ws, (int)(WS_PO + blk1 * 8192 + aw * 2048 + lane * 16), 0, SC1);
      __builtin_amdgcn_raw_buffer_store_b128(s, ws, (int)(WS_PO + blk1 * 8192 + aw * 2048 + 1024 + lane * 16), 0, SC1);
    }
    const __amdgpu_buffer_rsrc_t rx = RSRC(P.x[p1], 4 * 512 * 2);
    const __amdgpu_buffer_rsrc_t rq = RSRC(P.wq[p1], 512 * 4096 * 2);
    const __amdgpu_buffer_rsrc_t rk = RSRC(P.wuk[p1], 512 * 4096 * 2);
    v4u xf[4], wq[4][8], wk[2][4];
    const unsigned vq = g * 8 * 8192 + h * 256 + r * 16;
    const unsigned vk = r * 8192 + h * 256 + g * 16;
    const unsigned vx = (r >> 2) * 1024 + g * 16;
#pragma unroll
    for (int s = 0; s < 4; ++s) xf[s] = BLD(rx, vx, (128 * aw + 32 * ((s + cq) & 3)) * 2, 0);
#pragma unroll
    for (int jj = 0; jj < 8; ++jj) wq[0][jj] = BLD(rq, vq, (128 * aw + 32 * cq + jj) * 8192, 0);
#pragma unroll
    for (int t = 0; t < 2; ++t)
#pragma unroll
      for (int s = 0; s < 4; ++s) wk[t][s] = BLD(rk, vk + s * 64, (128 * cq + 32 * aw + 16 * t) * 8192, NT);
#pragma unroll
    for (int s = 1; s < 4; ++s)
#pragma unroll
      for (int jj = 0; jj < 8; ++jj) wq[s][jj] = BLD(rq, vq, (128 * aw + 32 * ((s + cq) & 3) + jj) * 8192, 0);
    __builtin_amdgcn_sched_barrier(0);
    asm volatile("s_waitcnt lgkmcnt(0)" ::: "memory");
    __builtin_amdgcn_s_barrier();
    __builtin_amdgcn_sched_barrier(0);
    STAMP(1);
    f32x4 acc[8];
#pragma unroll
    for (int i = 0; i < 8; ++i) acc[i] = f32x4{0.f, 0.f, 0.f, 0.f};
#pragma unroll
    for (int s = 0; s < 4; ++s)
#pragma unroll
      for (int i = 0; i < 8; ++i) acc[i] = mfma16(xf[s], tcol(wq[s], i), acc[i]);
    {
      v4u o = {pk(acc[0][0], acc[1][0]), pk(acc[2][0], acc[3][0]), pk(acc[4][0], acc[5][0]), pk(acc[6][0], acc[7][0])};
      *(LAS v4u*)(L + L_QP + (aw * 4 + g) * 272 + r * 16) = o;
    }
    STAMP(2);
    lds_signal(ctl + C_QP, lane);
    lds_wait(ctl + C_QP, 4);
    {
      f32x4 a2[2] = {f32x4{0.f, 0.f, 0.f, 0.f}, f32x4{0.f, 0.f, 0.f, 0.f}};
#pragma unroll
      for (int s = 0; s < 4; ++s) {
        const v4u qf = *(const LAS v4u*)(L + L_QP + ((r & 3) * 4 + (r >> 2)) * 272 + s * 64 + g * 16);
        a2[0] = mfma16(qf, wk[0][s], a2[0]);
        a2[1] = mfma16(qf, wk[1][s], a2[1]);
      }
#pragma unroll
      for (int t = 0; t < 2; ++t) {
        const float v = ((a2[t][0] + a2[t][1]) + (a2[t][2] + a2[t][3])) * QSCALE;
        *(LAS unsigned short*)(L + L_QA + g * 256 + (32 * aw + 16 * t + r) * 2) = (unsigned short)(pk(v, v) & 0xFFFFu);
      }
    }
    asm volatile("s_waitcnt vmcnt(0)" ::: "memory");
    lds_signal(ctl + C_QA, lane);
    if (aw == 0) {
      lds_wait(ctl + C_QA, 4);
      const v4u v = *(const LAS v4u*)(L + L_QA + lane * 16);
      const unsigned off = WS_QG + (((p1 * 4 + g) * 32 + 8 * cq + (r >> 1)) * 64 + (r & 1) * 32 + h) * 16;
      __builtin_amdgcn_raw_buffer_store_b128(v, ws, (int)off, 0, SC1);
      asm volatile("s_waitcnt vmcnt(0)" ::: "memory");
      if (lane < 4) __hip_atomic_store((gu32*)((char*)P.ws + WS_F1) + (p1 * 4 + lane) * 128 + src, 1u, RLX_AGENT);
      STAMP(3);
      for (unsigned spins = 0; spins < SPIN_CAP; ++spins) {
        const v2u f = __builtin_amdgcn_raw_buffer_load_b64(ws, (int)(WS_F1 + pb * 512 + lane * 8), 0, SC1);
        if (__all(f.x == 1u && f.y == 1u)) break;
        __builtin_amdgcn_s_sleep(2);
      }
      CFENCE();
      lds_signal(ctl + C_F1, lane);
    } else {
      lds_wait(ctl + C_F1, 1);
    }
    STAMP(4);
    v4u qf[32];
#pragma unroll
    for (int s = 0; s < 32; ++s) qf[s] = BLD(ws, lane * 16, WS_QG + (pb * 32 + s) * 1024, SC1);
    __builtin_amdgcn_sched_barrier(0);
    const int r32 = lane & 31, hi = lane >> 5;
    f32x16 sa;
#pragma unroll
    for (int i = 0; i < 16; ++i) sa[i] = 0.f;
    {
      const unsigned f = ((r32 & 3) << 2) | ((r32 >> 2) & 3);
      const unsigned rowb = (32 * aw + r32) * 256;
#pragma unroll
      for (int q = 0; q < 4; ++q) {
        lds_wait(ctl + C_KV0 + q, NK);
        if (q == 0) STAMP(5);
        if (q == 3) STAMP(6);
#pragma unroll
        for (int s8 = 0; s8 < 8; ++s8) {
          const v4u a = *(const LAS v4u*)(L + L_T + q * 32768 + rowb + (((2 * s8 + hi) ^ f) << 4));
          sa = mfma32(a, qf[8 * q + s8], sa);
        }
      }
    }
    float mx = sa[0];
#pragma unroll
    for (int i = 1; i < 16; ++i) mx = fmaxf(mx, sa[i]);
    mx = xor32_max(mx);
    if (hi == 0) *(LAS float*)(L + L_MX + r32 * 16 + aw * 4) = mx;
    lds_signal(ctl + C_MX, lane);
    lds_wait(ctl + C_MX, 4);
    {
      const f32x4 m4 = *(const LAS f32x4*)(L + L_MX + r32 * 16);
      mx = fmaxf(fmaxf(m4[0], m4[1]), fmaxf(m4[2], m4[3]));
    }
    {
      float ps = 0.f;
#pragma unroll
      for (int i = 0; i < 16; ++i) { sa[i] = __builtin_amdgcn_exp2f(sa[i] - mx); ps += sa[i]; }
      ps = xor32_sum(ps);
      const v4u p0 = {pk(sa[0], sa[1]), pk(sa[2], sa[3]), pk(sa[4], sa[5]), pk(sa[6], sa[7])};
      const v4u p1v = {pk(sa[8], sa[9]), pk(sa[10], sa[11]), pk(sa[12], sa[13]), pk(sa[14], sa[15])};
      *(LAS v4u*)(L + L_P + (2 * aw) * 1024 + lane * 16) = p0;
      *(LAS v4u*)(L + L_P + (2 * aw + 1) * 1024 + lane * 16) = p1v;
      if (hi == 0) *(LAS float*)(L + L_LS + r32 * 16 + aw * 4) = ps;
    }
    lds_signal(ctl + C_P, lane);
    lds_wait(ctl + C_P, 4);
    STAMP(7);
    f32x16 oa[4];
#pragma unroll
    for (int ct = 0; ct < 4; ++ct)
#pragma unroll
      for (int i = 0; i < 16; ++i) oa[ct][i] = 0.f;
    float lsum;
    {
      const f32x4 l4 = *(const LAS f32x4*)(L + L_LS + r32 * 16);
      lsum = (l4[0] + l4[1]) + (l4[2] + l4[3]);
      const int q4 = (lane & 15) >> 2, pp = lane & 3, blk = (lane >> 4) & 1;
#pragma unroll
      for (int ks = 0; ks < 8; ++ks) {
        const v4u pf = *(const LAS v4u*)(L + L_P + ks * 1024 + lane * 16);
#pragma unroll
        for (int ct = 0; ct < 4; ++ct) {
          s16x4 t2[2];
#pragma unroll
          for (int t = 0; t < 2; ++t) {
            const unsigned a = L_T + aw * 32768 + ks * 4096 + (8 * t + 4 * hi + q4) * 256 + ((ct ^ q4) << 6) +
                               (((2 * blk + (pp >> 1)) ^ ((2 * t + hi) & 3)) << 4) + ((pp & 1) << 3);
            t2[t] = __builtin_amdgcn_ds_read_tr16_b64_v4i16((LAS s16x4*)(L + a));
          }
          const bf16x8 af = __builtin_shufflevector(t2[0], t2[1], 0, 1, 2, 3, 4, 5, 6, 7);
          oa[ct] = mfma32(__builtin_bit_cast(v4u, af), pf, oa[ct]);
        }
      }
    }
    STAMP(8);
    asm volatile("s_waitcnt lgkmcnt(0)" ::: "memory");
#pragma unroll
    for (int ct = 0; ct < 4; ++ct)
#pragma unroll
      for (int gg = 0; gg < 4; ++gg) {
        const v2u v = {pk(oa[ct][4 * gg], oa[ct][4 * gg + 1]), pk(oa[ct][4 * gg + 2], oa[ct][4 * gg + 3])};
        *(LAS v2u*)(L + L_T + aw * 32768 + r32 * 256 + (((4 * ct + gg) ^ (r32 & 7)) << 4) + hi * 8) = v;
      }
    asm volatile("s_waitcnt lgkmcnt(0)" ::: "memory");
#pragma unroll
    for (int u = 0; u < 8; ++u) {
      const int hh = 4 * u + g;
      const v4u v = *(const LAS v4u*)(L + L_T + aw * 32768 + hh * 256 + ((r ^ (hh & 7)) << 4));
      const unsigned off = WS_OG + ((p2 * 128 + hh * 4 + aw) * 128 + b2 * 32 + j) * 256 + r * 16;
      __builtin_amdgcn_raw_buffer_store_b128(v, ws, (int)off, 0, SC1);
    }
    asm volatile("s_waitcnt vmcnt(0)" ::: "memory");
    if (hi == 0) {
      const v2u gr = {__float_as_uint(mx), __float_as_uint(lsum) | 1u};
      __builtin_amdgcn_raw_buffer_store_b64(gr, ws, (int)(WS_F2 + ((p2 * 128 + r32 * 4 + aw) * 128 + b2 * 32 + j) * 8), 0, SC1);
    }
    STAMP(9);
    {
      v4u st[4];
      const unsigned fo = WS_F2 + blk1 * 1024 + aw * 256 + g * 64;
      for (unsigned spins = 0; spins < SPIN_CAP; ++spins) {
#pragma unroll
        for (int k = 0; k < 4; ++k) st[k] = BLD(ws, fo + k * 16, 0, SC1);
        bool ok = true;
#pragma unroll
        for (int k = 0; k < 4; ++k) ok = ok && (st[k].y != 0u) && (st[k].w != 0u);
        if (__all(ok)) break;
        __builtin_amdgcn_s_sleep(1);
      }
      CFENCE();
      STAMP(10);
      v4u od[8];
#pragma unroll
      for (int i = 0; i < 8; ++i) od[i] = BLD(ws, (8 * g + i) * 256 + r * 16, WS_OG + blk1 * 32768 + aw * 8192, SC1);
      __builtin_amdgcn_sched_barrier(0);
      float m[8], l[8];
#pragma unroll
      for (int k = 0; k < 4; ++k) {
        m[2 * k] = __uint_as_float(st[k].x); l[2 * k] = __uint_as_float(st[k].y);
        m[2 * k + 1] = __uint_as_float(st[k].z); l[2 * k + 1] = __uint_as_float(st[k].w);
      }
      float M = m[0];
#pragma unroll
      for (int i = 1; i < 8; ++i) M = fmaxf(M, m[i]);
      M = xor32_max(xor16_max(M));
      float Ls = 0.f;
#pragma unroll
      for (int i = 0; i < 8; ++i) { m[i] = __builtin_amdgcn_exp2f(m[i] - M); Ls += m[i] * l[i]; }
      Ls = xor32_sum(xor16_sum(Ls));
      const float inv = 1.0f / Ls;
      v4u wf;
#pragma unroll
      for (int k = 0; k < 4; ++k) {
        const float w0 = m[2 * k] * inv, w1 = m[2 * k + 1] * inv;
        const unsigned hi2 = pk(w0, w1);
        const unsigned lo2 = pk(w0 - __uint_as_float(hi2 << 16), w1 - __uint_as_float(hi2 & 0xFFFF0000u));
        wf[k] = (r & 1) ? lo2 : hi2;
      }
      f32x4 la[8];
#pragma unroll
      for (int e = 0; e < 8; ++e) la[e] = mfma16(wf, tcol(od, e), f32x4{0.f, 0.f, 0.f, 0.f});
      const v4u o = {pk(la[0][0] + la[0][1], la[1][0] + la[1][1]), pk(la[2][0] + la[2][1], la[3][0] + la[3][1]),
                     pk(la[4][0] + la[4][1], la[5][0] + la[5][1]), pk(la[6][0] + la[6][1], la[7][0] + la[7][1])};
      if (g == 0) *(LAS v4u*)(L + L_QA + aw * 256 + r * 16) = o;
      lds_signal(ctl + C_LAT, lane);
      STAMP(11);
    }
    if (aw == 0) {
      v4u pv[8];
      const unsigned po = WS_PO + blk1 * 8192 + lane * 16;
      for (unsigned spins = 0; spins < SPIN_CAP; ++spins) {
#pragma unroll
        for (int k = 0; k < 8; ++k) pv[k] = BLD(ws, po + k * 1024, 0, SC1);
        bool ok = true;
#pragma unroll
        for (int k = 0; k < 8; ++k) ok = ok && pv[k].x != SENT && pv[k].y != SENT && pv[k].z != SENT && pv[k].w != SENT;
        if (__all(ok)) break;
        __builtin_amdgcn_s_sleep(1);
      }
      STAMP(12);
      f32x4 s = __builtin_bit_cast(f32x4, pv[0]);
#pragma unroll
      for (int k = 1; k < 8; ++k) s += __builtin_bit_cast(f32x4, pv[k]);
#pragma unroll
      for (int c = 0; c < 4; ++c) {
        float v = s[c];
        v += __shfl_xor(v, 4); v += __shfl_xor(v, 8);
        v = xor32_sum(xor16_sum(v));
        s[c] = v;
      }
      if (lane < 4) {
        const int bo = src >> 5, m0 = (src & 31) * 16 + lane * 4;
        const v2u o = {pk(s[0], s[1]), pk(s[2], s[3])};
        *(GAS v2u*)((char*)P.out + (bo * 1024 + p1 * 512 + m0) * 2) = o;
      }
      STAMP(13);
    }
  } else {
    const int lw = wave - 4;
    asm volatile("s_waitcnt lgkmcnt(0)" ::: "memory");
    __builtin_amdgcn_s_barrier();
    if (lw < NK) {
      const char* kvb = (const char*)P.ckv[p2] + ((size_t)b2 * 4096 + 128 * j) * 1024;
      unsigned vo[4];
#pragma unroll
      for (int u3 = 0; u3 < 4; ++u3) vo[u3] = g * 1024 + ((r ^ ((g << 2) | u3)) << 4);
      constexpr int PER = 32 / NK;
#pragma unroll
      for (int q = 0; q < 4; ++q) {
#pragma unroll
        for (int i = 0; i < PER; ++i) {
          const int u = i * NK + lw;
          const int u3 = (NK == 1) ? (i & 3) : 0;
          unsigned v = (NK == 1) ? vo[u3] : (unsigned)(g * 1024 + ((r ^ ((g << 2) | (u & 3))) << 4));
          dma16(kvb + u * 4096 + q * 256, v, (unsigned)(size_t)(L + L_T) + q * 32768 + u * 1024);
          if (NK == 1) {
            const int n = q * 32 + i + 1;
            if (n == 80) { asm volatile("s_waitcnt vmcnt(48)" ::: "memory"); lds_signal(ctl + C_KV0, lane); }
            if (n == 112) { asm volatile("s_waitcnt vmcnt(48)" ::: "memory"); lds_signal(ctl + C_KV1, lane); }
          }
        }
      }
      lds_signal(ctl + C_ISS, lane);
      STAMP(1);
      if (NK == 1) {
        asm volatile("s_waitcnt vmcnt(32)" ::: "memory"); lds_signal(ctl + C_KV2, lane);
      } else if (NK == 2) {
        asm volatile("s_waitcnt vmcnt(48)" ::: "memory"); lds_signal(ctl + C_KV0, lane);
        asm volatile("s_waitcnt vmcnt(32)" ::: "memory"); lds_signal(ctl + C_KV1, lane);
        asm volatile("s_waitcnt vmcnt(16)" ::: "memory"); lds_signal(ctl + C_KV2, lane);
      } else {
        asm volatile("s_waitcnt vmcnt(24)" ::: "memory"); lds_signal(ctl + C_KV0, lane);
        asm volatile("s_waitcnt vmcnt(16)" ::: "memory"); lds_signal(ctl + C_KV1, lane);
        asm volatile("s_waitcnt vmcnt(8)" ::: "memory"); lds_signal(ctl + C_KV2, lane);
      }
      asm volatile("s_waitcnt vmcnt(0)" ::: "memory");
      lds_signal(ctl + C_KV3, lane);
      STAMP(2);
    } else {
      lds_wait(ctl + C_ISS, NK);
    }
    const __amdgpu_buffer_rsrc_t rv = RSRC(P.wuv[p1], 512 * 4096 * 2);
    const __amdgpu_buffer_rsrc_t ro = RSRC(P.wo[p1], 512 * 4096 * 2);
    v4u wv[8], wo[4][8];
    const unsigned vv = g * 8 * 8192 + h * 256 + r * 16;
    const unsigned vo2 = g * 8 * 1024 + r * 16;
#pragma unroll
    for (int s = 0; s < 1; ++s)
#pragma unroll
      for (int jj = 0; jj < 8; ++jj) wo[s][jj] = BLD(ro, vo2, (h * 128 + 32 * ((s + cq) & 3) + jj) * 1024 + lw * 256, 0);
#pragma unroll
    for (int jj = 0; jj < 8; ++jj) wv[jj] = BLD(rv, vv, (128 * cq + 32 * lw + jj) * 8192, NT);
#pragma unroll
    for (int s = 1; s < 4; ++s)
#pragma unroll
      for (int jj = 0; jj < 8; ++jj) wo[s][jj] = BLD(ro, vo2, (h * 128 + 32 * ((s + cq) & 3) + jj) * 1024 + lw * 256, 0);
    __builtin_amdgcn_sched_barrier(0);
    STAMP(3);
    v4u tv[8], to[4][8];
#pragma unroll
    for (int i = 0; i < 8; ++i) { tv[i] = tcol(wv, i); asm volatile("" : "+v"(tv[i])); }
#pragma unroll
    for (int s = 0; s < 4; ++s)
#pragma unroll
      for (int i = 0; i < 8; ++i) { to[s][i] = tcol(wo[s], i); asm volatile("" : "+v"(to[s][i])); }
    STAMP(4);
    lds_wait(ctl + C_LAT, 4);
    {
      const v4u lf = *(const LAS v4u*)(L + L_QA + (r >> 2) * 256 + lw * 64 + g * 16);
      f32x4 ca[8];
#pragma unroll
      for (int i = 0; i < 8; ++i) ca[i] = mfma16(lf, tv[i], f32x4{0.f, 0.f, 0.f, 0.f});
      const v4u o = {pk(ca[0][0], ca[1][0]), pk(ca[2][0], ca[3][0]), pk(ca[4][0], ca[5][0]), pk(ca[6][0], ca[7][0])};
      *(LAS v4u*)(L + L_QP + (lw * 4 + g) * 272 + r * 16) = o;
    }
    lds_signal(ctl + C_CTX, lane);
    lds_wait(ctl + C_CTX, 4);
    STAMP(5);
    f32x4 oa[8];
#pragma unroll
    for (int i = 0; i < 8; ++i) oa[i] = f32x4{0.f, 0.f, 0.f, 0.f};
#pragma unroll
    for (int s = 0; s < 4; ++s) {
      const v4u cf = *(const LAS v4u*)(L + L_QP + ((r & 3) * 4 + (r >> 2)) * 272 + ((s + cq) & 3) * 64 + g * 16);
#pragma unroll
      for (int i = 0; i < 8; ++i) oa[i] = mfma16(cf, to[s][i], oa[i]);
    }
    v4u o0, o1;
#pragma unroll
    for (int i = 0; i < 8; ++i) {
      unsigned u = __float_as_uint((oa[i][0] + oa[i][1]) + (oa[i][2] + oa[i][3]));
      u = (u == SENT) ? 0x7FC00000u : u;
      if (i < 4) o0[i] = u; else o1[i - 4] = u;
    }
    const unsigned po = WS_PO + ((p1 * 128 + 32 * g + 8 * lw + (r >> 1)) * 128 + src) * 64 + (r & 1) * 32;
    __builtin_amdgcn_raw_buffer_store_b128(o0, ws, (int)po, 0, SC1);
    __builtin_amdgcn_raw_buffer_store_b128(o1, ws, (int)(po + 16), 0, SC1);
    STAMP(6);
  }
}

extern "C" void kernel_launch(void* const* d_in, const int* in_sizes, int n_in, void* d_out, int out_size, void* d_ws,
                              size_t ws_size, hipStream_t stream) {
  Params P;
  for (int p = 0; p < 2; ++p) {
    P.x[p] = d_in[6 * p + 0]; P.ckv[p] = d_in[6 * p + 1]; P.wq[p] = d_in[6 * p + 2];
    P.wuk[p] = d_in[6 * p + 3]; P.wuv[p] = d_in[6 * p + 4]; P.wo[p] = d_in[6 * p + 5];
  }
  P.out = d_out; P.ws = d_ws;
  (void)hipFuncSetAttribute((const void*)mla_fused, hipFuncAttributeMaxDynamicSharedMemorySize, (int)L_END);
  (void)hipMemsetAsync((char*)d_ws + WS_F1, 0, WS_F1_BYTES, stream);
  mla_fused<<<256, 512, L_END, stream>>>(P);
}
```

```cpp
#include <hip/hip_runtime.h>
#include <hip/hip_bf16.h>

#define GAS __attribute__((address_space(1)))
#define LAS __attribute__((address_space(3)))
typedef unsigned v4u __attribute__((ext_vector_type(4)));
typedef unsigned v2u __attribute__((ext_vector_type(2)));
typedef float f32x4 __attribute__((ext_vector_type(4)));
typedef float f32x16 __attribute__((ext_vector_type(16)));
typedef short bf16x8 __attribute__((ext_vector_type(8)));
typedef short s16x4 __attribute__((ext_vector_type(4)));
typedef GAS unsigned gu32;
#define RLX_AGENT __ATOMIC_RELAXED, __HIP_MEMORY_SCOPE_AGENT
#define RLX_WG __ATOMIC_RELAXED, __HIP_MEMORY_SCOPE_WORKGROUP
#define DI __device__ __forceinline__

#define KDEPTH 32
#define KEARLY 0
#define WDEPTH 8
#define STAMPS 0

constexpr unsigned WS_F1 = 0;
constexpr unsigned WS_AA = 4096;
constexpr unsigned WS_RQ = 5120;
constexpr unsigned WS_RA = 6144;
constexpr unsigned WS_QG = 8192;
constexpr unsigned WS_F2 = WS_QG + 8 * 32768;
constexpr unsigned WS_OG = WS_F2 + 256 * 1024;
constexpr unsigned WS_PO = WS_OG + 256 * 32768;
constexpr unsigned WS_ST = WS_PO + 256 * 8192;
constexpr unsigned WS_END = WS_ST + 256 * 8 * 32 * 8;

constexpr unsigned L_T = 0;
constexpr unsigned L_QP = 131072;
constexpr unsigned L_QA = L_QP + 16 * 272;
constexpr unsigned L_P = L_QA + 1024;
constexpr unsigned L_MX = L_P + 8192;
constexpr unsigned L_LS = L_MX + 512;
constexpr unsigned L_CT = L_LS + 512;
constexpr unsigned L_END = L_CT + 128;
enum { C_QP = 0, C_QA, C_F1, C_KV0, C_KV1, C_KV2, C_KV3, C_ISS, C_MX, C_P, C_LAT, C_CTX, C_ACK, C_WST, C_ABI, C_N };

constexpr unsigned SENT = 0xFFFFFFFFu;
constexpr unsigned SPIN_CAP = 1u << 16;
constexpr float QSCALE = 0.08838834764831845f * 1.4426950408889634f;

struct Params {
  const void* x[2]; const void* ckv[2]; const void* wq[2]; const void* wuk[2]; const void* wuv[2]; const void* wo[2];
  void* out; void* ws;
};

DI f32x4 mfma16(v4u a, v4u b, f32x4 c) {
  return __builtin_amdgcn_mfma_f32_16x16x32_bf16(__builtin_bit_cast(bf16x8, a), __builtin_bit_cast(bf16x8, b), c, 0, 0, 0);
}
DI f32x16 mfma32(v4u a, v4u b, f32x16 c) {
  return __builtin_amdgcn_mfma_f32_32x32x16_bf16(__builtin_bit_cast(bf16x8, a), __builtin_bit_cast(bf16x8, b), c, 0, 0, 0);
}
typedef __bf16 bf2_t __attribute__((ext_vector_type(2)));
typedef float f2_t __attribute__((ext_vector_type(2)));
DI unsigned pk(float lo, float hi) {
  f2_t f = {lo, hi};
  return __builtin_bit_cast(unsigned, __builtin_convertvector(f, bf2_t));
}
DI v4u tcol(const v4u (&w)[8], int i) {
  const unsigned sel = (i & 1) ? 0x07060302u : 0x05040100u;
  v4u o;
  o.x = __builtin_amdgcn_perm(w[1][i >> 1], w[0][i >> 1], sel);
  o.y = __builtin_amdgcn_perm(w[3][i >> 1], w[2][i >> 1], sel);
  o.z = __builtin_amdgcn_perm(w[5][i >> 1], w[4][i >> 1], sel);
  o.w = __builtin_amdgcn_perm(w[7][i >> 1], w[6][i >> 1], sel);
  return o;
}
#define CFENCE() asm volatile("" ::: "memory")
#define SB() __builtin_amdgcn_sched_barrier(0)
DI void lds_signal(LAS unsigned* c, int lane) {
  CFENCE();
  if (lane == 0) __hip_atomic_fetch_add(c, 1u, RLX_WG);
  CFENCE();
}
DI void lds_wait_slow(LAS unsigned* c, unsigned target) {
  CFENCE();
  while ((unsigned)__builtin_amdgcn_readfirstlane(__hip_atomic_load(c, RLX_WG)) < target) __builtin_amdgcn_s_sleep(2);
  CFENCE();
}
DI void lds_wait(LAS unsigned* c, unsigned target) {
  CFENCE();
  while ((unsigned)__builtin_amdgcn_readfirstlane(__hip_atomic_load(c, RLX_WG)) < target) {}
  CFENCE();
}
DI float xor32_max(float v) {
  auto r = __builtin_amdgcn_permlane32_swap(__float_as_uint(v), __float_as_uint(v), false, false);
  return fmaxf(__uint_as_float(r[0]), __uint_as_float(r[1]));
}
DI float xor32_sum(float v) {
  auto r = __builtin_amdgcn_permlane32_swap(__float_as_uint(v), __float_as_uint(v), false, false);
  return __uint_as_float(r[0]) + __uint_as_float(r[1]);
}
DI float xor16_max(float v) {
  auto r = __builtin_amdgcn_permlane16_swap(__float_as_uint(v), __float_as_uint(v), false, false);
  return fmaxf(__uint_as_float(r[0]), __uint_as_float(r[1]));
}
DI float xor16_sum(float v) {
  auto r = __builtin_amdgcn_permlane16_swap(__float_as_uint(v), __float_as_uint(v), false, false);
  return __uint_as_float(r[0]) + __uint_as_float(r[1]);
}
DI void dma16(const void* sbase, unsigned voff, unsigned lds_dst) {
  unsigned keep;
  asm volatile("s_mov_b32 %0, m0\n\ts_mov_b32 m0, %3\n\ts_nop 0\n\tglobal_load_lds_dwordx4 %1, %2 nt\n\ts_mov_b32 m0, %0"
               : "=&s"(keep) : "v"(voff), "s"(sbase), "s"(lds_dst) : "memory");
}
template <int N> DI void wait_vm() { asm volatile("s_waitcnt vmcnt(%0)" ::"i"(N) : "memory"); }
#define STAMP(i) do {} while (0)
#define TS(i) do {} while (0)
#define RSRC(ptr, bytes) __builtin_amdgcn_make_buffer_rsrc((void*)(ptr), 0, (int)(bytes), 0x00020000)
#define BLD(rs, vo, so, aux) __builtin_amdgcn_raw_buffer_load_b128(rs, (int)(vo), (int)(so), aux)
#define BST(v, rs, vo) __builtin_amdgcn_raw_buffer_store_b128(v, rs, (int)(vo), 0, SC1)
constexpr int SC1 = 16, NT = 2;

__global__ void __launch_bounds__(512, 2) mla_fused(Params P) {
  extern __shared__ __attribute__((aligned(16))) unsigned char smem[];
  LAS unsigned char* L = (LAS unsigned char*)smem;
  LAS unsigned* ctl = (LAS unsigned*)(L + L_CT);
  const int tid = threadIdx.x, lane = tid & 63;
  const int wave = __builtin_amdgcn_readfirstlane(tid >> 6);
  const int w = blockIdx.x;
  const int p1 = (w >> 5) & 1, h = w & 31, cq = w >> 6;
  const int src = h * 4 + cq;
  const int blk1 = p1 * 128 + src;
  const int pb = w & 7, p2 = pb >> 2, b2 = pb & 3, j = w >> 3;
  const int r = lane & 15, g = lane >> 4;
  const __amdgpu_buffer_rsrc_t ws = RSRC(P.ws, WS_END);
  TS(0);
  if (tid < C_N) ctl[tid] = 0u;
  asm volatile("s_waitcnt lgkmcnt(0)" ::: "memory");
  __builtin_amdgcn_s_barrier();

  if (wave < 4) {
    const int aw = wave;
    const __amdgpu_buffer_rsrc_t rx = RSRC(P.x[p1], 4 * 512 * 2);
    const __amdgpu_buffer_rsrc_t rq = RSRC(P.wq[p1], 512 * 4096 * 2);
    const __amdgpu_buffer_rsrc_t rk = RSRC(P.wuk[p1], 512 * 4096 * 2);
    v4u xf[4], wq[4][8], wk[2][4];
    const unsigned vq = g * 8 * 8192 + h * 256 + r * 16;
    const unsigned vk = r * 8192 + h * 256 + g * 16;
    const unsigned vx = (r >> 2) * 1024 + g * 16;
#pragma unroll
    for (int jj = 0; jj < 8; ++jj) wq[0][jj] = BLD(rq, vq, (128 * aw + 32 * cq + jj) * 8192, NT);
#pragma unroll
    for (int t = 0; t < 2; ++t)
#pragma unroll
      for (int s = 0; s < 4; ++s) wk[t][s] = BLD(rk, vk + s * 64, (128 * cq + 32 * aw + 16 * t) * 8192, NT);
#pragma unroll
    for (int s = 0; s < 4; ++s) xf[s] = BLD(rx, vx, (128 * aw + 32 * ((s + cq) & 3)) * 2, 0);
    {
      const v4u z = {0u, 0u, 0u, 0u}, s = {SENT, SENT, SENT, SENT};
      if (aw == 0) BST(z, ws, WS_F2 + blk1 * 1024 + lane * 16);
      BST(s, ws, WS_PO + blk1 * 8192 + aw * 2048 + lane * 16);
      BST(s, ws, WS_PO + blk1 * 8192 + aw * 2048 + 1024 + lane * 16);
    }
#pragma unroll
    for (int s = 1; s < 4; ++s)
#pragma unroll
      for (int jj = 0; jj < 8; ++jj) wq[s][jj] = BLD(rq, vq, (128 * aw + 32 * ((s + cq) & 3) + jj) * 8192, NT);
    SB();
    if (!KEARLY) lds_signal(ctl + C_ABI, lane);
    TS(1);
    f32x4 acc[8];
#pragma unroll
    for (int i = 0; i < 8; ++i) acc[i] = f32x4{0.f, 0.f, 0.f, 0.f};
#pragma unroll
    for (int s = 0; s < 4; ++s)
#pragma unroll
      for (int i = 0; i < 8; ++i) acc[i] = mfma16(xf[s], tcol(wq[s], i), acc[i]);
    {
      v4u o = {pk(acc[0][0], acc[1][0]), pk(acc[2][0], acc[3][0]), pk(acc[4][0], acc[5][0]), pk(acc[6][0], acc[7][0])};
      *(LAS v4u*)(L + L_QP + (aw * 4 + g) * 272 + r * 16) = o;
    }
    lds_signal(ctl + C_QP, lane);
    TS(2);
    lds_wait(ctl + C_QP, 4);
    {
      f32x4 a2[2] = {f32x4{0.f, 0.f, 0.f, 0.f}, f32x4{0.f, 0.f, 0.f, 0.f}};
#pragma unroll
      for (int s = 0; s < 4; ++s) {
        const v4u qf = *(const LAS v4u*)(L + L_QP + ((r & 3) * 4 + (r >> 2)) * 272 + s * 64 + g * 16);
        a2[0] = mfma16(qf, wk[0][s], a2[0]);
        a2[1] = mfma16(qf, wk[1][s], a2[1]);
      }
#pragma unroll
      for (int t = 0; t < 2; ++t) {
        const float v = ((a2[t][0] + a2[t][1]) + (a2[t][2] + a2[t][3])) * QSCALE;
        *(LAS unsigned short*)(L + L_QA + g * 256 + (32 * aw + 16 * t + r) * 2) = (unsigned short)(pk(v, v) & 0xFFFFu);
      }
    }
    wait_vm<0>();
    lds_signal(ctl + C_QA, lane);
    if (aw == 0) {
      lds_wait(ctl + C_QA, 4);
      const v4u v = *(const LAS v4u*)(L + L_QA + lane * 16);
      const unsigned off = WS_QG + (((p1 * 4 + g) * 32 + 8 * cq + (r >> 1)) * 64 + (r & 1) * 32 + h) * 16;
      BST(v, ws, off);
      TS(14);
      wait_vm<0>();
      lds_wait(ctl + C_ACK, 1);
      if (lane < 4) __hip_atomic_store((gu32*)((char*)P.ws + WS_F1) + (p1 * 4 + lane) * 128 + src, 1u, RLX_AGENT);
      TS(3);
      for (unsigned spins = 0; spins < SPIN_CAP; ++spins) {
        CFENCE();
        const v2u f = __builtin_amdgcn_raw_buffer_load_b64(ws, (int)(WS_F1 + pb * 512 + lane * 8), 0, SC1);
        if (__all(f.x == 1u && f.y == 1u)) break;
        __builtin_amdgcn_s_sleep(2);
      }
      lds_signal(ctl + C_F1, lane);
    } else {
      lds_wait_slow(ctl + C_F1, 1);
    }
    TS(4);
    v4u qf[32];
#pragma unroll
    for (int s = 0; s < 32; ++s) qf[s] = BLD(ws, lane * 16, WS_QG + (pb * 32 + s) * 1024, SC1);
    SB();
    const int r32 = lane & 31, hi = lane >> 5;
    f32x16 sa;
#pragma unroll
    for (int i = 0; i < 16; ++i) sa[i] = 0.f;
    {
      const unsigned f = ((r32 & 3) << 2) | ((r32 >> 2) & 3);
      const unsigned rowb = (32 * aw + r32) * 256;
#pragma unroll
      for (int q = 0; q < 4; ++q) {
        lds_wait(ctl + C_KV0 + q, 1);
        if (q == 0) TS(5);
        if (q == 3) TS(6);
        v4u a[8];
#pragma unroll
        for (int s8 = 0; s8 < 8; ++s8) a[s8] = *(const LAS v4u*)(L + L_T + q * 32768 + rowb + (((2 * s8 + hi) ^ f) << 4));
        SB();
#pragma unroll
        for (int s8 = 0; s8 < 8; ++s8) sa = mfma32(a[s8], qf[8 * q + s8], sa);
        SB();
      }
    }
    float mx = sa[0];
#pragma unroll
    for (int i = 1; i < 16; ++i) mx = fmaxf(mx, sa[i]);
    mx = xor32_max(mx);
    if (hi == 0) *(LAS float*)(L + L_MX + r32 * 16 + aw * 4) = mx;
    lds_signal(ctl + C_MX, lane);
    lds_wait(ctl + C_MX, 4);
    {
      const f32x4 m4 = *(const LAS f32x4*)(L + L_MX + r32 * 16);
      mx = fmaxf(fmaxf(m4[0], m4[1]), fmaxf(m4[2], m4[3]));
    }
    s16x4 tf[8][4][2];
    const int q4 = (lane & 15) >> 2, pp = lane & 3, blk = (lane >> 4) & 1;
#define TRADDR(ks, ct, t) (L_T + aw * 32768 + (ks) * 4096 + (8 * (t) + 4 * hi + q4) * 256 + (((ct) ^ q4) << 6) + \
                           (((2 * blk + (pp >> 1)) ^ ((2 * (t) + hi) & 3)) << 4) + ((pp & 1) << 3))
    SB();
#pragma unroll
    for (int ks = 0; ks < 4; ++ks)
#pragma unroll
      for (int ct = 0; ct < 4; ++ct)
#pragma unroll
        for (int t = 0; t < 2; ++t) tf[ks][ct][t] = __builtin_amdgcn_ds_read_tr16_b64_v4i16((LAS s16x4*)(L + TRADDR(ks, ct, t)));
    SB();
    {
      float ps = 0.f;
#pragma unroll
      for (int i = 0; i < 16; ++i) { sa[i] = __builtin_amdgcn_exp2f(sa[i] - mx); ps += sa[i]; }
      ps = xor32_sum(ps);
      const v4u p0 = {pk(sa[0], sa[1]), pk(sa[2], sa[3]), pk(sa[4], sa[5]), pk(sa[6], sa[7])};
      const v4u p1v = {pk(sa[8], sa[9]), pk(sa[10], sa[11]), pk(sa[12], sa[13]), pk(sa[14], sa[15])};
      *(LAS v4u*)(L + L_P + (2 * aw) * 1024 + lane * 16) = p0;
      *(LAS v4u*)(L + L_P + (2 * aw + 1) * 1024 + lane * 16) = p1v;
      if (hi == 0) *(LAS float*)(L + L_LS + r32 * 16 + aw * 4) = ps;
    }
    lds_signal(ctl + C_P, lane);
    lds_wait(ctl + C_P, 4);
    TS(7);
    f32x16 oa[4];
#pragma unroll
    for (int ct = 0; ct < 4; ++ct)
#pragma unroll
      for (int i = 0; i < 16; ++i) oa[ct][i] = 0.f;
    v4u pf[8];
#pragma unroll
    for (int ks = 0; ks < 8; ++ks) pf[ks] = *(const LAS v4u*)(L + L_P + ks * 1024 + lane * 16);
    const f32x4 l4 = *(const LAS f32x4*)(L + L_LS + r32 * 16);
#pragma unroll
    for (int ks = 4; ks < 8; ++ks)
#pragma unroll
      for (int ct = 0; ct < 4; ++ct)
#pragma unroll
        for (int t = 0; t < 2; ++t) tf[ks][ct][t] = __builtin_amdgcn_ds_read_tr16_b64_v4i16((LAS s16x4*)(L + TRADDR(ks, ct, t)));
    SB();
#pragma unroll
    for (int ks = 0; ks < 8; ++ks)
#pragma unroll
      for (int ct = 0; ct < 4; ++ct) {
        const bf16x8 af = __builtin_shufflevector(tf[ks][ct][0], tf[ks][ct][1], 0, 1, 2, 3, 4, 5, 6, 7);
        oa[ct] = mfma32(__builtin_bit_cast(v4u, af), pf[ks], oa[ct]);
      }
    const float lsum = (l4[0] + l4[1]) + (l4[2] + l4[3]);
    TS(8);
#pragma unroll
    for (int ct = 0; ct < 4; ++ct)
#pragma unroll
      for (int gg = 0; gg < 4; ++gg) {
        const v2u v = {pk(oa[ct][4 * gg], oa[ct][4 * gg + 1]), pk(oa[ct][4 * gg + 2], oa[ct][4 * gg + 3])};
        *(LAS v2u*)(L + L_T + aw * 32768 + r32 * 256 + (((4 * ct + gg) ^ (r32 & 7)) << 4) + hi * 8) = v;
      }
    CFENCE();
#pragma unroll
    for (int u = 0; u < 8; ++u) {
      const int hh = 4 * u + g;
      const v4u v = *(const LAS v4u*)(L + L_T + aw * 32768 + hh * 256 + ((r ^ (hh & 7)) << 4));
      const unsigned off = WS_OG + ((p2 * 128 + hh * 4 + aw) * 128 + b2 * 32 + j) * 256 + r * 16;
      BST(v, ws, off);
    }
    TS(15);
    wait_vm<0>();
    if (hi == 0) {
      const v2u gr = {__float_as_uint(mx), __float_as_uint(lsum) | 1u};
      __builtin_amdgcn_raw_buffer_store_b64(gr, ws, (int)(WS_F2 + ((p2 * 128 + r32 * 4 + aw) * 128 + b2 * 32 + j) * 8), 0, SC1);
    }
    TS(9);
    {
      v4u st[4];
      const unsigned fo = WS_F2 + blk1 * 1024 + aw * 256 + g * 64;
      for (unsigned spins = 0; spins < SPIN_CAP; ++spins) {
        CFENCE();
#pragma unroll
        for (int k = 0; k < 4; ++k) st[k] = BLD(ws, fo + k * 16, 0, SC1);
        bool ok = true;
#pragma unroll
        for (int k = 0; k < 4; ++k) ok = ok && (st[k].y != 0u) && (st[k].w != 0u);
        if (__all(ok)) break;
      }
      CFENCE();
      TS(10);
      v4u od[8];
#pragma unroll
      for (int i = 0; i < 8; ++i) od[i] = BLD(ws, (8 * g + i) * 256 + r * 16, WS_OG + blk1 * 32768 + aw * 8192, SC1);
      SB();
      float m[8], l[8];
#pragma unroll
      for (int k = 0; k < 4; ++k) {
        m[2 * k] = __uint_as_float(st[k].x); l[2 * k] = __uint_as_float(st[k].y);
        m[2 * k + 1] = __uint_as_float(st[k].z); l[2 * k + 1] = __uint_as_float(st[k].w);
      }
      float M = m[0];
#pragma unroll
      for (int i = 1; i < 8; ++i) M = fmaxf(M, m[i]);
      M = xor32_max(xor16_max(M));
      float Ls = 0.f;
#pragma unroll
      for (int i = 0; i < 8; ++i) { m[i] = __builtin_amdgcn_exp2f(m[i] - M); Ls += m[i] * l[i]; }
      Ls = xor32_sum(xor16_sum(Ls));
      const float inv = 1.0f / Ls;
      v4u wf;
#pragma unroll
      for (int k = 0; k < 4; ++k) {
        const float w0 = m[2 * k] * inv, w1 = m[2 * k + 1] * inv;
        const unsigned hi2 = pk(w0, w1);
        const unsigned lo2 = pk(w0 - __uint_as_float(hi2 << 16), w1 - __uint_as_float(hi2 & 0xFFFF0000u));
        wf[k] = (r & 1) ? lo2 : hi2;
      }
      SB();
      f32x4 la[8];
#pragma unroll
      for (int e = 0; e < 8; ++e) la[e] = mfma16(wf, tcol(od, e), f32x4{0.f, 0.f, 0.f, 0.f});
      const v4u o = {pk(la[0][0] + la[0][1], la[1][0] + la[1][1]), pk(la[2][0] + la[2][1], la[3][0] + la[3][1]),
                     pk(la[4][0] + la[4][1], la[5][0] + la[5][1]), pk(la[6][0] + la[6][1], la[7][0] + la[7][1])};
      if (g == 0) *(LAS v4u*)(L + L_QA + aw * 256 + r * 16) = o;
      lds_signal(ctl + C_LAT, lane);
      TS(11);
    }
    if (aw == 0) {
      lds_wait_slow(ctl + C_WST, 4);
      v4u pv[8];
      const unsigned po = WS_PO + blk1 * 8192 + lane * 16;
      for (unsigned spins = 0; spins < SPIN_CAP; ++spins) {
        CFENCE();
#pragma unroll
        for (int k = 0; k < 8; ++k) pv[k] = BLD(ws, po + k * 1024, 0, SC1);
        bool ok = true;
#pragma unroll
        for (int k = 0; k < 8; ++k) ok = ok && pv[k].x != SENT && pv[k].y != SENT && pv[k].z != SENT && pv[k].w != SENT;
        if (__all(ok)) break;
      }
      TS(12);
      f32x4 s = __builtin_bit_cast(f32x4, pv[0]);
#pragma unroll
      for (int k = 1; k < 8; ++k) s += __builtin_bit_cast(f32x4, pv[k]);
#pragma unroll
      for (int c = 0; c < 4; ++c) {
        float v = s[c];
        v += __shfl_xor(v, 4); v += __shfl_xor(v, 8);
        v = xor32_sum(xor16_sum(v));
        s[c] = v;
      }
      if (lane < 4) {
        const int bo = src >> 5, m0 = (src & 31) * 16 + lane * 4;
        const v2u o = {pk(s[0], s[1]), pk(s[2], s[3])};
        *(GAS v2u*)((char*)P.out + (bo * 1024 + p1 * 512 + m0) * 2) = o;
      }
      TS(13);
    }
  } else {
    const int lw = wave - 4;
    if (lw == 0) {
      if (!KEARLY) lds_wait_slow(ctl + C_ABI, 4);
      const char* kvb = (const char*)P.ckv[p2] + ((size_t)b2 * 4096 + 128 * j) * 1024;
      unsigned vo[4];
#pragma unroll
      for (int u3 = 0; u3 < 4; ++u3) vo[u3] = g * 1024 + ((r ^ ((g << 2) | u3)) << 4);
#pragma unroll
      for (int G = 0; G < 16; ++G) {
        if (8 * G >= KDEPTH) {
          wait_vm<KDEPTH - 8>();
          const int done = 8 * G - (KDEPTH - 8);
          if (done >= 32 && done - 8 < 32) lds_signal(ctl + C_KV0, lane);
          if (done >= 64 && done - 8 < 64) lds_signal(ctl + C_KV1, lane);
          if (done >= 96 && done - 8 < 96) lds_signal(ctl + C_KV2, lane);
        }
#pragma unroll
        for (int i = 0; i < 8; ++i) {
          const int n = 8 * G + i, q = n >> 5, u = n & 31;
          dma16(kvb + u * 4096 + q * 256, vo[u & 3], (unsigned)(size_t)(L + L_T) + q * 32768 + u * 1024);
        }
      }
      lds_signal(ctl + C_ISS, lane);
      TS(1);
      if (120 - (KDEPTH - 8) < 96) { wait_vm<32>(); lds_signal(ctl + C_KV2, lane); }
      wait_vm<0>();
      lds_signal(ctl + C_KV3, lane);
      TS(2);
    } else {
      if (lw == 3) {
        gu32* ra = (gu32*)((char*)P.ws + WS_RA) + w;
        const unsigned r0 = __hip_atomic_load(ra, RLX_AGENT);
        if (lane == 0) __hip_atomic_store((gu32*)((char*)P.ws + WS_RQ) + w, r0, RLX_AGENT);
        for (unsigned spins = 0; spins < SPIN_CAP; ++spins) {
        CFENCE();
          if ((unsigned)__builtin_amdgcn_readfirstlane(__hip_atomic_load(ra, RLX_AGENT)) != (unsigned)__builtin_amdgcn_readfirstlane(r0)) break;
          __builtin_amdgcn_s_sleep(2);
        }
        CFENCE();
        if (lane == 0) __hip_atomic_store((gu32*)((char*)P.ws + WS_AA) + w, 1u, RLX_AGENT);
        lds_signal(ctl + C_ACK, lane);
        TS(7);
      } else if (lw == 2 && w == 0) {
        v4u a = BLD(ws, WS_RA + lane * 16, 0, SC1);
        const v4u z = {0u, 0u, 0u, 0u};
#pragma unroll
        for (int k = 0; k < 5; ++k) BST(z, ws, WS_F1 + k * 1024 + lane * 16);
        wait_vm<0>();
        BST(a + 1u, ws, WS_RA + lane * 16);
        for (unsigned spins = 0; spins < SPIN_CAP; ++spins) {
        CFENCE();
          const v4u aa = BLD(ws, WS_AA + lane * 16, 0, SC1);
          const v4u rq = BLD(ws, WS_RQ + lane * 16, 0, SC1);
          if (__all(aa.x == 1u && aa.y == 1u && aa.z == 1u && aa.w == 1u)) break;
          BST(rq + 1u, ws, WS_RA + lane * 16);
        }
        TS(8);
      }
      lds_wait_slow(ctl + C_ISS, 1);
    }
    const __amdgpu_buffer_rsrc_t rv = RSRC(P.wuv[p1], 512 * 4096 * 2);
    const __amdgpu_buffer_rsrc_t ro = RSRC(P.wo[p1], 512 * 4096 * 2);
    v4u wv[8], wo[4][8];
    const unsigned vv = g * 8 * 8192 + h * 256 + r * 16;
    const unsigned vo2 = g * 8 * 1024 + r * 16;
#pragma unroll
    for (int jj = 0; jj < 8; ++jj) wo[0][jj] = BLD(ro, vo2, (h * 128 + 32 * cq + jj) * 1024 + lw * 256, NT);
    SB(); if (WDEPTH < 8) wait_vm<WDEPTH < 63 ? WDEPTH : 63>();
#pragma unroll
    for (int jj = 0; jj < 8; ++jj) wv[jj] = BLD(rv, vv, (128 * cq + 32 * lw + jj) * 8192, NT);
#pragma unroll
    for (int s = 1; s < 4; ++s) {
      SB(); if (WDEPTH < 8 * (s + 1)) wait_vm<WDEPTH < 63 ? WDEPTH : 63>();
#pragma unroll
      for (int jj = 0; jj < 8; ++jj) wo[s][jj] = BLD(ro, vo2, (h * 128 + 32 * ((s + cq) & 3) + jj) * 1024 + lw * 256, NT);
    }
    SB();
    TS(3);
    v4u tv[8], to[4][8];
#pragma unroll
    for (int i = 0; i < 8; ++i) { to[0][i] = tcol(wo[0], i); asm volatile("" : "+v"(to[0][i])); }
#pragma unroll
    for (int i = 0; i < 8; ++i) { tv[i] = tcol(wv, i); asm volatile("" : "+v"(tv[i])); }
#pragma unroll
    for (int s = 1; s < 4; ++s)
#pragma unroll
      for (int i = 0; i < 8; ++i) { to[s][i] = tcol(wo[s], i); asm volatile("" : "+v"(to[s][i])); }
    TS(4);
    lds_wait(ctl + C_LAT, 4);
    {
      const v4u lf = *(const LAS v4u*)(L + L_QA + (r >> 2) * 256 + lw * 64 + g * 16);
      f32x4 ca[8];
#pragma unroll
      for (int i = 0; i < 8; ++i) ca[i] = mfma16(lf, tv[i], f32x4{0.f, 0.f, 0.f, 0.f});
      const v4u o = {pk(ca[0][0], ca[1][0]), pk(ca[2][0], ca[3][0]), pk(ca[4][0], ca[5][0]), pk(ca[6][0], ca[7][0])};
      *(LAS v4u*)(L + L_QP + (lw * 4 + g) * 272 + r * 16) = o;
    }
    lds_signal(ctl + C_CTX, lane);
    lds_wait(ctl + C_CTX, 4);
    TS(5);
    f32x4 oa[8];
#pragma unroll
    for (int i = 0; i < 8; ++i) oa[i] = f32x4{0.f, 0.f, 0.f, 0.f};
#pragma unroll
    for (int s = 0; s < 4; ++s) {
      const v4u cf = *(const LAS v4u*)(L + L_QP + ((r & 3) * 4 + (r >> 2)) * 272 + ((s + cq) & 3) * 64 + g * 16);
#pragma unroll
      for (int i = 0; i < 8; ++i) oa[i] = mfma16(cf, to[s][i], oa[i]);
    }
    v4u o0, o1;
#pragma unroll
    for (int i = 0; i < 8; ++i) {
      unsigned u = __float_as_uint((oa[i][0] + oa[i][1]) + (oa[i][2] + oa[i][3]));
      u = (u == SENT) ? 0x7FC00000u : u;
      if (i < 4) o0[i] = u; else o1[i - 4] = u;
    }
    const unsigned po = WS_PO + ((p1 * 128 + 32 * g + 8 * lw + (r >> 1)) * 128 + src) * 64 + (r & 1) * 32;
    BST(o0, ws, po);
    BST(o1, ws, po + 16);
    lds_signal(ctl + C_WST, lane);
    TS(6);
  }
}

extern "C" void kernel_launch(void* const* d_in, const int* in_sizes, int n_in, void* d_out, int out_size, void* d_ws,
                              size_t ws_size, hipStream_t stream) {
  Params P;
  for (int p = 0; p < 2; ++p) {
    P.x[p] = d_in[6 * p + 0]; P.ckv[p] = d_in[6 * p + 1]; P.wq[p] = d_in[6 * p + 2];
    P.wuk[p] = d_in[6 * p + 3]; P.wuv[p] = d_in[6 * p + 4]; P.wo[p] = d_in[6 * p + 5];
  }
  P.out = d_out; P.ws = d_ws;
  (void)hipFuncSetAttribute((const void*)mla_fused, hipFuncAttributeMaxDynamicSharedMemorySize, (int)L_END);
  mla_fused<<<256, 512, L_END, stream>>>(P);
}
```

```cpp
#include <hip/hip_runtime.h>
#include <hip/hip_bf16.h>

#define GAS __attribute__((address_space(1)))
#define LAS __attribute__((address_space(3)))
typedef unsigned v4u __attribute__((ext_vector_type(4)));
typedef unsigned v2u __attribute__((ext_vector_type(2)));
typedef float f32x4 __attribute__((ext_vector_type(4)));
typedef float f32x16 __attribute__((ext_vector_type(16)));
typedef short bf16x8 __attribute__((ext_vector_type(8)));
typedef short s16x4 __attribute__((ext_vector_type(4)));
typedef GAS unsigned gu32;
#define RLX_AGENT __ATOMIC_RELAXED, __HIP_MEMORY_SCOPE_AGENT
#define RLX_WG __ATOMIC_RELAXED, __HIP_MEMORY_SCOPE_WORKGROUP
#define DI __device__ __forceinline__

#define KDEPTH 32
#define KEARLY 0
#define WDEPTH 8
#define STAMPS 1
#include <cstdio>

constexpr unsigned WS_F1 = 0;
constexpr unsigned WS_AA = 4096;
constexpr unsigned WS_RQ = 5120;
constexpr unsigned WS_RA = 6144;
constexpr unsigned WS_QG = 8192;
constexpr unsigned WS_F2 = WS_QG + 8 * 32768;
constexpr unsigned WS_OG = WS_F2 + 256 * 1024;
constexpr unsigned WS_PO = WS_OG + 256 * 32768;
constexpr unsigned WS_ST = WS_PO + 256 * 8192;
constexpr unsigned WS_END = WS_ST + 256 * 8 * 32 * 8;

constexpr unsigned L_T = 0;
constexpr unsigned L_QP = 131072;
constexpr unsigned L_QA = L_QP + 16 * 272;
constexpr unsigned L_P = L_QA + 1024;
constexpr unsigned L_MX = L_P + 8192;
constexpr unsigned L_LS = L_MX + 512;
constexpr unsigned L_CT = L_LS + 512;
constexpr unsigned L_END = L_CT + 128;
enum { C_QP = 0, C_QA, C_F1, C_KV0, C_KV1, C_KV2, C_KV3, C_ISS, C_MX, C_P, C_LAT, C_CTX, C_ACK, C_WST, C_ABI, C_N };

constexpr unsigned SENT = 0xFFFFFFFFu;
constexpr unsigned SPIN_CAP = 1u << 16;
constexpr float QSCALE = 0.08838834764831845f * 1.4426950408889634f;

struct Params {
  const void* x[2]; const void* ckv[2]; const void* wq[2]; const void* wuk[2]; const void* wuv[2]; const void* wo[2];
  void* out; void* ws;
};

DI f32x4 mfma16(v4u a, v4u b, f32x4 c) {
  return __builtin_amdgcn_mfma_f32_16x16x32_bf16(__builtin_bit_cast(bf16x8, a), __builtin_bit_cast(bf16x8, b), c, 0, 0, 0);
}
DI f32x16 mfma32(v4u a, v4u b, f32x16 c) {
  return __builtin_amdgcn_mfma_f32_32x32x16_bf16(__builtin_bit_cast(bf16x8, a), __builtin_bit_cast(bf16x8, b), c, 0, 0, 0);
}
typedef __bf16 bf2_t __attribute__((ext_vector_type(2)));
typedef float f2_t __attribute__((ext_vector_type(2)));
DI unsigned pk(float lo, float hi) {
  f2_t f = {lo, hi};
  return __builtin_bit_cast(unsigned, __builtin_convertvector(f, bf2_t));
}
DI v4u tcol(const v4u (&w)[8], int i) {
  const unsigned sel = (i & 1) ? 0x07060302u : 0x05040100u;
  v4u o;
  o.x = __builtin_amdgcn_perm(w[1][i >> 1], w[0][i >> 1], sel);
  o.y = __builtin_amdgcn_perm(w[3][i >> 1], w[2][i >> 1], sel);
  o.z = __builtin_amdgcn_perm(w[5][i >> 1], w[4][i >> 1], sel);
  o.w = __builtin_amdgcn_perm(w[7][i >> 1], w[6][i >> 1], sel);
  return o;
}
#define CFENCE() asm volatile("" ::: "memory")
#define SB() __builtin_amdgcn_sched_barrier(0)
DI void lds_signal(LAS unsigned* c, int lane) {
  CFENCE();
  if (lane == 0) __hip_atomic_fetch_add(c, 1u, RLX_WG);
  CFENCE();
}
DI void lds_wait_slow(LAS unsigned* c, unsigned target) {
  CFENCE();
  while ((unsigned)__builtin_amdgcn_readfirstlane(__hip_atomic_load(c, RLX_WG)) < target) __builtin_amdgcn_s_sleep(2);
  CFENCE();
}
DI void lds_wait(LAS unsigned* c, unsigned target) {
  CFENCE();
  while ((unsigned)__builtin_amdgcn_readfirstlane(__hip_atomic_load(c, RLX_WG)) < target) {}
  CFENCE();
}
DI float xor32_max(float v) {
  auto r = __builtin_amdgcn_permlane32_swap(__float_as_uint(v), __float_as_uint(v), false, false);
  return fmaxf(__uint_as_float(r[0]), __uint_as_float(r[1]));
}
DI float xor32_sum(float v) {
  auto r = __builtin_amdgcn_permlane32_swap(__float_as_uint(v), __float_as_uint(v), false, false);
  return __uint_as_float(r[0]) + __uint_as_float(r[1]);
}
DI float xor16_max(float v) {
  auto r = __builtin_amdgcn_permlane16_swap(__float_as_uint(v), __float_as_uint(v), false, false);
  return fmaxf(__uint_as_float(r[0]), __uint_as_float(r[1]));
}
DI float xor16_sum(float v) {
  auto r = __builtin_amdgcn_permlane16_swap(__float_as_uint(v), __float_as_uint(v), false, false);
  return __uint_as_float(r[0]) + __uint_as_float(r[1]);
}
DI void dma16(const void* sbase, unsigned voff, unsigned lds_dst) {
  unsigned keep;
  asm volatile("s_mov_b32 %0, m0\n\ts_mov_b32 m0, %3\n\ts_nop 0\n\tglobal_load_lds_dwordx4 %1, %2 nt\n\ts_mov_b32 m0, %0"
               : "=&s"(keep) : "v"(voff), "s"(sbase), "s"(lds_dst) : "memory");
}
template <int N> DI void wait_vm() { asm volatile("s_waitcnt vmcnt(%0)" ::"i"(N) : "memory"); }
#define STAMP(i) do { if (lane == 0) ((GAS unsigned long long*)((char*)P.ws + WS_ST))[(blockIdx.x * 8 + wave) * 32 + (i)] = tstamp[i]; } while (0)
#define TS(i) do { SB(); tstamp[i] = __builtin_amdgcn_s_memrealtime(); SB(); } while (0)
#define RSRC(ptr, bytes) __builtin_amdgcn_make_buffer_rsrc((void*)(ptr), 0, (int)(bytes), 0x00020000)
#define BLD(rs, vo, so, aux) __builtin_amdgcn_raw_buffer_load_b128(rs, (int)(vo), (int)(so), aux)
#define BST(v, rs, vo) __builtin_amdgcn_raw_buffer_store_b128(v, rs, (int)(vo), 0, SC1)
constexpr int SC1 = 16, NT = 2;

__global__ void __launch_bounds__(512, 2) mla_fused(Params P) {
  extern __shared__ __attribute__((aligned(16))) unsigned char smem[];
  LAS unsigned char* L = (LAS unsigned char*)smem;
  LAS unsigned* ctl = (LAS unsigned*)(L + L_CT);
  const int tid = threadIdx.x, lane = tid & 63;
  const int wave = __builtin_amdgcn_readfirstlane(tid >> 6);
  const int w = blockIdx.x;
  const int p1 = (w >> 5) & 1, h = w & 31, cq = w >> 6;
  const int src = h * 4 + cq;
  const int blk1 = p1 * 128 + src;
  const int pb = w & 7, p2 = pb >> 2, b2 = pb & 3, j = w >> 3;
  const int r = lane & 15, g = lane >> 4;
  const __amdgpu_buffer_rsrc_t ws = RSRC(P.ws, WS_END);
  unsigned long long tstamp[16];
#pragma unroll
  for (int i = 0; i < 16; ++i) tstamp[i] = 0;
  TS(0);
  if (tid < C_N) ctl[tid] = 0u;
  asm volatile("s_waitcnt lgkmcnt(0)" ::: "memory");
  __builtin_amdgcn_s_barrier();

  if (wave < 4) {
    const int aw = wave;
    const __amdgpu_buffer_rsrc_t rx = RSRC(P.x[p1], 4 * 512 * 2);
    const __amdgpu_buffer_rsrc_t rq = RSRC(P.wq[p1], 512 * 4096 * 2);
    const __amdgpu_buffer_rsrc_t rk = RSRC(P.wuk[p1], 512 * 4096 * 2);
    v4u xf[4], wq[4][8], wk[2][4];
    const unsigned vq = g * 8 * 8192 + h * 256 + r * 16;
    const unsigned vk = r * 8192 + h * 256 + g * 16;
    const unsigned vx = (r >> 2) * 1024 + g * 16;
#pragma unroll
    for (int jj = 0; jj < 8; ++jj) wq[0][jj] = BLD(rq, vq, (128 * aw + 32 * cq + jj) * 8192, NT);
#pragma unroll
    for (int t = 0; t < 2; ++t)
#pragma unroll
      for (int s = 0; s < 4; ++s) wk[t][s] = BLD(rk, vk + s * 64, (128 * cq + 32 * aw + 16 * t) * 8192, NT);
#pragma unroll
    for (int s = 0; s < 4; ++s) xf[s] = BLD(rx, vx, (128 * aw + 32 * ((s + cq) & 3)) * 2, 0);
    {
      const v4u z = {0u, 0u, 0u, 0u}, s = {SENT, SENT, SENT, SENT};
      if (aw == 0) BST(z, ws, WS_F2 + blk1 * 1024 + lane * 16);
      BST(s, ws, WS_PO + blk1 * 8192 + aw * 2048 + lane * 16);
      BST(s, ws, WS_PO + blk1 * 8192 + aw * 2048 + 1024 + lane * 16);
    }
#pragma unroll
    for (int s = 1; s < 4; ++s)
#pragma unroll
      for (int jj = 0; jj < 8; ++jj) wq[s][jj] = BLD(rq, vq, (128 * aw + 32 * ((s + cq) & 3) + jj) * 8192, NT);
    SB();
    if (!KEARLY) lds_signal(ctl + C_ABI, lane);
    TS(1);
    f32x4 acc[8];
#pragma unroll
    for (int i = 0; i < 8; ++i) acc[i] = f32x4{0.f, 0.f, 0.f, 0.f};
#pragma unroll
    for (int s = 0; s < 4; ++s)
#pragma unroll
      for (int i = 0; i < 8; ++i) acc[i] = mfma16(xf[s], tcol(wq[s], i), acc[i]);
    {
      v4u o = {pk(acc[0][0], acc[1][0]), pk(acc[2][0], acc[3][0]), pk(acc[4][0], acc[5][0]), pk(acc[6][0], acc[7][0])};
      *(LAS v4u*)(L + L_QP + (aw * 4 + g) * 272 + r * 16) = o;
    }
    lds_signal(ctl + C_QP, lane);
    TS(2);
    lds_wait(ctl + C_QP, 4);
    {
      f32x4 a2[2] = {f32x4{0.f, 0.f, 0.f, 0.f}, f32x4{0.f, 0.f, 0.f, 0.f}};
#pragma unroll
      for (int s = 0; s < 4; ++s) {
        const v4u qf = *(const LAS v4u*)(L + L_QP + ((r & 3) * 4 + (r >> 2)) * 272 + s * 64 + g * 16);
        a2[0] = mfma16(qf, wk[0][s], a2[0]);
        a2[1] = mfma16(qf, wk[1][s], a2[1]);
      }
#pragma unroll
      for (int t = 0; t < 2; ++t) {
        const float v = ((a2[t][0] + a2[t][1]) + (a2[t][2] + a2[t][3])) * QSCALE;
        *(LAS unsigned short*)(L + L_QA + g * 256 + (32 * aw + 16 * t + r) * 2) = (unsigned short)(pk(v, v) & 0xFFFFu);
      }
    }
    wait_vm<0>();
    lds_signal(ctl + C_QA, lane);
    if (aw == 0) {
      lds_wait(ctl + C_QA, 4);
      const v4u v = *(const LAS v4u*)(L + L_QA + lane * 16);
      const unsigned off = WS_QG + (((p1 * 4 + g) * 32 + 8 * cq + (r >> 1)) * 64 + (r & 1) * 32 + h) * 16;
      BST(v, ws, off);
      TS(14);
      wait_vm<0>();
      lds_wait(ctl + C_ACK, 1);
      if (lane < 4) __hip_atomic_store((gu32*)((char*)P.ws + WS_F1) + (p1 * 4 + lane) * 128 + src, 1u, RLX_AGENT);
      TS(3);
      for (unsigned spins = 0; spins < SPIN_CAP; ++spins) {
        CFENCE();
        const v2u f = __builtin_amdgcn_raw_buffer_load_b64(ws, (int)(WS_F1 + pb * 512 + lane * 8), 0, SC1);
        if (__all(f.x == 1u && f.y == 1u)) break;
        __builtin_amdgcn_s_sleep(2);
      }
      lds_signal(ctl + C_F1, lane);
    } else {
      lds_wait_slow(ctl + C_F1, 1);
    }
    TS(4);
    v4u qf[32];
#pragma unroll
    for (int s = 0; s < 32; ++s) qf[s] = BLD(ws, lane * 16, WS_QG + (pb * 32 + s) * 1024, SC1);
    SB();
    const int r32 = lane & 31, hi = lane >> 5;
    f32x16 sa;
#pragma unroll
    for (int i = 0; i < 16; ++i) sa[i] = 0.f;
    {
      const unsigned f = ((r32 & 3) << 2) | ((r32 >> 2) & 3);
      const unsigned rowb = (32 * aw + r32) * 256;
#pragma unroll
      for (int q = 0; q < 4; ++q) {
        lds_wait(ctl + C_KV0 + q, 1);
        if (q == 0) TS(5);
        if (q == 3) TS(6);
        v4u a[8];
#pragma unroll
        for (int s8 = 0; s8 < 8; ++s8) a[s8] = *(const LAS v4u*)(L + L_T + q * 32768 + rowb + (((2 * s8 + hi) ^ f) << 4));
        SB();
#pragma unroll
        for (int s8 = 0; s8 < 8; ++s8) sa = mfma32(a[s8], qf[8 * q + s8], sa);
        SB();
      }
    }
    float mx = sa[0];
#pragma unroll
    for (int i = 1; i < 16; ++i) mx = fmaxf(mx, sa[i]);
    mx = xor32_max(mx);
    if (hi == 0) *(LAS float*)(L + L_MX + r32 * 16 + aw * 4) = mx;
    lds_signal(ctl + C_MX, lane);
    lds_wait(ctl + C_MX, 4);
    {
      const f32x4 m4 = *(const LAS f32x4*)(L + L_MX + r32 * 16);
      mx = fmaxf(fmaxf(m4[0], m4[1]), fmaxf(m4[2], m4[3]));
    }
    s16x4 tf[8][4][2];
    const int q4 = (lane & 15) >> 2, pp = lane & 3, blk = (lane >> 4) & 1;
#define TRADDR(ks, ct, t) (L_T + aw * 32768 + (ks) * 4096 + (8 * (t) + 4 * hi + q4) * 256 + (((ct) ^ q4) << 6) + \
                           (((2 * blk + (pp >> 1)) ^ ((2 * (t) + hi) & 3)) << 4) + ((pp & 1) << 3))
    SB();
#pragma unroll
    for (int ks = 0; ks < 4; ++ks)
#pragma unroll
      for (int ct = 0; ct < 4; ++ct)
#pragma unroll
        for (int t = 0; t < 2; ++t) tf[ks][ct][t] = __builtin_amdgcn_ds_read_tr16_b64_v4i16((LAS s16x4*)(L + TRADDR(ks, ct, t)));
    SB();
    {
      float ps = 0.f;
#pragma unroll
      for (int i = 0; i < 16; ++i) { sa[i] = __builtin_amdgcn_exp2f(sa[i] - mx); ps += sa[i]; }
      ps = xor32_sum(ps);
      const v4u p0 = {pk(sa[0], sa[1]), pk(sa[2], sa[3]), pk(sa[4], sa[5]), pk(sa[6], sa[7])};
      const v4u p1v = {pk(sa[8], sa[9]), pk(sa[10], sa[11]), pk(sa[12], sa[13]), pk(sa[14], sa[15])};
      *(LAS v4u*)(L + L_P + (2 * aw) * 1024 + lane * 16) = p0;
      *(LAS v4u*)(L + L_P + (2 * aw + 1) * 1024 + lane * 16) = p1v;
      if (hi == 0) *(LAS float*)(L + L_LS + r32 * 16 + aw * 4) = ps;
    }
    lds_signal(ctl + C_P, lane);
    lds_wait(ctl + C_P, 4);
    TS(7);
    f32x16 oa[4];
#pragma unroll
    for (int ct = 0; ct < 4; ++ct)
#pragma unroll
      for (int i = 0; i < 16; ++i) oa[ct][i] = 0.f;
    v4u pf[8];
#pragma unroll
    for (int ks = 0; ks < 8; ++ks) pf[ks] = *(const LAS v4u*)(L + L_P + ks * 1024 + lane * 16);
    const f32x4 l4 = *(const LAS f32x4*)(L + L_LS + r32 * 16);
#pragma unroll
    for (int ks = 4; ks < 8; ++ks)
#pragma unroll
      for (int ct = 0; ct < 4; ++ct)
#pragma unroll
        for (int t = 0; t < 2; ++t) tf[ks][ct][t] = __builtin_amdgcn_ds_read_tr16_b64_v4i16((LAS s16x4*)(L + TRADDR(ks, ct, t)));
    SB();
#pragma unroll
    for (int ks = 0; ks < 8; ++ks)
#pragma unroll
      for (int ct = 0; ct < 4; ++ct) {
        const bf16x8 af = __builtin_shufflevector(tf[ks][ct][0], tf[ks][ct][1], 0, 1, 2, 3, 4, 5, 6, 7);
        oa[ct] = mfma32(__builtin_bit_cast(v4u, af), pf[ks], oa[ct]);
      }
    const float lsum = (l4[0] + l4[1]) + (l4[2] + l4[3]);
    TS(8);
#pragma unroll
    for (int ct = 0; ct < 4; ++ct)
#pragma unroll
      for (int gg = 0; gg < 4; ++gg) {
        const v2u v = {pk(oa[ct][4 * gg], oa[ct][4 * gg + 1]), pk(oa[ct][4 * gg + 2], oa[ct][4 * gg + 3])};
        *(LAS v2u*)(L + L_T + aw * 32768 + r32 * 256 + (((4 * ct + gg) ^ (r32 & 7)) << 4) + hi * 8) = v;
      }
    CFENCE();
#pragma unroll
    for (int u = 0; u < 8; ++u) {
      const int hh = 4 * u + g;
      const v4u v = *(const LAS v4u*)(L + L_T + aw * 32768 + hh * 256 + ((r ^ (hh & 7)) << 4));
      const unsigned off = WS_OG + ((p2 * 128 + hh * 4 + aw) * 128 + b2 * 32 + j) * 256 + r * 16;
      BST(v, ws, off);
    }
    TS(15);
    wait_vm<0>();
    if (hi == 0) {
      const v2u gr = {__float_as_uint(mx), __float_as_uint(lsum) | 1u};
      __builtin_amdgcn_raw_buffer_store_b64(gr, ws, (int)(WS_F2 + ((p2 * 128 + r32 * 4 + aw) * 128 + b2 * 32 + j) * 8), 0, SC1);
    }
    TS(9);
    {
      v4u st[4];
      const unsigned fo = WS_F2 + blk1 * 1024 + aw * 256 + g * 64;
      for (unsigned spins = 0; spins < SPIN_CAP; ++spins) {
        CFENCE();
#pragma unroll
        for (int k = 0; k < 4; ++k) st[k] = BLD(ws, fo + k * 16, 0, SC1);
        bool ok = true;
#pragma unroll
        for (int k = 0; k < 4; ++k) ok = ok && (st[k].y != 0u) && (st[k].w != 0u);
        if (__all(ok)) break;
      }
      CFENCE();
      TS(10);
      v4u od[8];
#pragma unroll
      for (int i = 0; i < 8; ++i) od[i] = BLD(ws, (8 * g + i) * 256 + r * 16, WS_OG + blk1 * 32768 + aw * 8192, SC1);
      SB();
      float m[8], l[8];
#pragma unroll
      for (int k = 0; k < 4; ++k) {
        m[2 * k] = __uint_as_float(st[k].x); l[2 * k] = __uint_as_float(st[k].y);
        m[2 * k + 1] = __uint_as_float(st[k].z); l[2 * k + 1] = __uint_as_float(st[k].w);
      }
      float M = m[0];
#pragma unroll
      for (int i = 1; i < 8; ++i) M = fmaxf(M, m[i]);
      M = xor32_max(xor16_max(M));
      float Ls = 0.f;
#pragma unroll
      for (int i = 0; i < 8; ++i) { m[i] = __builtin_amdgcn_exp2f(m[i] - M); Ls += m[i] * l[i]; }
      Ls = xor32_sum(xor16_sum(Ls));
      const float inv = 1.0f / Ls;
      v4u wf;
#pragma unroll
      for (int k = 0; k < 4; ++k) {
        const float w0 = m[2 * k] * inv, w1 = m[2 * k + 1] * inv;
        const unsigned hi2 = pk(w0, w1);
        const unsigned lo2 = pk(w0 - __uint_as_float(hi2 << 16), w1 - __uint_as_float(hi2 & 0xFFFF0000u));
        wf[k] = (r & 1) ? lo2 : hi2;
      }
      SB();
      f32x4 la[8];
#pragma unroll
      for (int e = 0; e < 8; ++e) la[e] = mfma16(wf, tcol(od, e), f32x4{0.f, 0.f, 0.f, 0.f});
      const v4u o = {pk(la[0][0] + la[0][1], la[1][0] + la[1][1]), pk(la[2][0] + la[2][1], la[3][0] + la[3][1]),
                     pk(la[4][0] + la[4][1], la[5][0] + la[5][1]), pk(la[6][0] + la[6][1], la[7][0] + la[7][1])};
      if (g == 0) *(LAS v4u*)(L + L_QA + aw * 256 + r * 16) = o;
      lds_signal(ctl + C_LAT, lane);
      TS(11);
    }
    if (aw == 0) {
      lds_wait_slow(ctl + C_WST, 4);
      v4u pv[8];
      const unsigned po = WS_PO + blk1 * 8192 + lane * 16;
      for (unsigned spins = 0; spins < SPIN_CAP; ++spins) {
        CFENCE();
#pragma unroll
        for (int k = 0; k < 8; ++k) pv[k] = BLD(ws, po + k * 1024, 0, SC1);
        bool ok = true;
#pragma unroll
        for (int k = 0; k < 8; ++k) ok = ok && pv[k].x != SENT && pv[k].y != SENT && pv[k].z != SENT && pv[k].w != SENT;
        if (__all(ok)) break;
      }
      TS(12);
      f32x4 s = __builtin_bit_cast(f32x4, pv[0]);
#pragma unroll
      for (int k = 1; k < 8; ++k) s += __builtin_bit_cast(f32x4, pv[k]);
#pragma unroll
      for (int c = 0; c < 4; ++c) {
        float v = s[c];
        v += __shfl_xor(v, 4); v += __shfl_xor(v, 8);
        v = xor32_sum(xor16_sum(v));
        s[c] = v;
      }
      if (lane < 4) {
        const int bo = src >> 5, m0 = (src & 31) * 16 + lane * 4;
        const v2u o = {pk(s[0], s[1]), pk(s[2], s[3])};
        *(GAS v2u*)((char*)P.out + (bo * 1024 + p1 * 512 + m0) * 2) = o;
      }
      TS(13);
    }
  } else {
    const int lw = wave - 4;
    if (lw == 0) {
      if (!KEARLY) lds_wait_slow(ctl + C_ABI, 4);
      const char* kvb = (const char*)P.ckv[p2] + ((size_t)b2 * 4096 + 128 * j) * 1024;
      unsigned vo[4];
#pragma unroll
      for (int u3 = 0; u3 < 4; ++u3) vo[u3] = g * 1024 + ((r ^ ((g << 2) | u3)) << 4);
#pragma unroll
      for (int G = 0; G < 16; ++G) {
        if (8 * G >= KDEPTH) {
          wait_vm<KDEPTH - 8>();
          const int done = 8 * G - (KDEPTH - 8);
          if (done >= 32 && done - 8 < 32) lds_signal(ctl + C_KV0, lane);
          if (done >= 64 && done - 8 < 64) lds_signal(ctl + C_KV1, lane);
          if (done >= 96 && done - 8 < 96) lds_signal(ctl + C_KV2, lane);
        }
#pragma unroll
        for (int i = 0; i < 8; ++i) {
          const int n = 8 * G + i, q = n >> 5, u = n & 31;
          dma16(kvb + u * 4096 + q * 256, vo[u & 3], (unsigned)(size_t)(L + L_T) + q * 32768 + u * 1024);
        }
      }
      lds_signal(ctl + C_ISS, lane);
      TS(1);
      if (120 - (KDEPTH - 8) < 96) { wait_vm<32>(); lds_signal(ctl + C_KV2, lane); }
      wait_vm<0>();
      lds_signal(ctl + C_KV3, lane);
      TS(2);
    } else {
      if (lw == 3) {
        gu32* ra = (gu32*)((char*)P.ws + WS_RA) + w;
        const unsigned r0 = __hip_atomic_load(ra, RLX_AGENT);
        if (lane == 0) __hip_atomic_store((gu32*)((char*)P.ws + WS_RQ) + w, r0, RLX_AGENT);
        for (unsigned spins = 0; spins < SPIN_CAP; ++spins) {
        CFENCE();
          if ((unsigned)__builtin_amdgcn_readfirstlane(__hip_atomic_load(ra, RLX_AGENT)) != (unsigned)__builtin_amdgcn_readfirstlane(r0)) break;
          __builtin_amdgcn_s_sleep(2);
        }
        CFENCE();
        if (lane == 0) __hip_atomic_store((gu32*)((char*)P.ws + WS_AA) + w, 1u, RLX_AGENT);
        lds_signal(ctl + C_ACK, lane);
        TS(7);
      } else if (lw == 2 && w == 0) {
        v4u a = BLD(ws, WS_RA + lane * 16, 0, SC1);
        const v4u z = {0u, 0u, 0u, 0u};
#pragma unroll
        for (int k = 0; k < 5; ++k) BST(z, ws, WS_F1 + k * 1024 + lane * 16);
        wait_vm<0>();
        BST(a + 1u, ws, WS_RA + lane * 16);
        for (unsigned spins = 0; spins < SPIN_CAP; ++spins) {
        CFENCE();
          const v4u aa = BLD(ws, WS_AA + lane * 16, 0, SC1);
          const v4u rq = BLD(ws, WS_RQ + lane * 16, 0, SC1);
          if (__all(aa.x == 1u && aa.y == 1u && aa.z == 1u && aa.w == 1u)) break;
          BST(rq + 1u, ws, WS_RA + lane * 16);
        }
        TS(8);
      }
      lds_wait_slow(ctl + C_ISS, 1);
    }
    const __amdgpu_buffer_rsrc_t rv = RSRC(P.wuv[p1], 512 * 4096 * 2);
    const __amdgpu_buffer_rsrc_t ro = RSRC(P.wo[p1], 512 * 4096 * 2);
    v4u wv[8], wo[4][8];
    const unsigned vv = g * 8 * 8192 + h * 256 + r * 16;
    const unsigned vo2 = g * 8 * 1024 + r * 16;
#pragma unroll
    for (int jj = 0; jj < 8; ++jj) wo[0][jj] = BLD(ro, vo2, (h * 128 + 32 * cq + jj) * 1024 + lw * 256, NT);
    SB(); if (WDEPTH < 8) wait_vm<WDEPTH < 63 ? WDEPTH : 63>();
#pragma unroll
    for (int jj = 0; jj < 8; ++jj) wv[jj] = BLD(rv, vv, (128 * cq + 32 * lw + jj) * 8192, NT);
#pragma unroll
    for (int s = 1; s < 4; ++s) {
      SB(); if (WDEPTH < 8 * (s + 1)) wait_vm<WDEPTH < 63 ? WDEPTH : 63>();
#pragma unroll
      for (int jj = 0; jj < 8; ++jj) wo[s][jj] = BLD(ro, vo2, (h * 128 + 32 * ((s + cq) & 3) + jj) * 1024 + lw * 256, NT);
    }
    SB();
    TS(3);
    v4u tv[8], to[4][8];
#pragma unroll
    for (int i = 0; i < 8; ++i) { to[0][i] = tcol(wo[0], i); asm volatile("" : "+v"(to[0][i])); }
#pragma unroll
    for (int i = 0; i < 8; ++i) { tv[i] = tcol(wv, i); asm volatile("" : "+v"(tv[i])); }
#pragma unroll
    for (int s = 1; s < 4; ++s)
#pragma unroll
      for (int i = 0; i < 8; ++i) { to[s][i] = tcol(wo[s], i); asm volatile("" : "+v"(to[s][i])); }
    TS(4);
    lds_wait(ctl + C_LAT, 4);
    {
      const v4u lf = *(const LAS v4u*)(L + L_QA + (r >> 2) * 256 + lw * 64 + g * 16);
      f32x4 ca[8];
#pragma unroll
      for (int i = 0; i < 8; ++i) ca[i] = mfma16(lf, tv[i], f32x4{0.f, 0.f, 0.f, 0.f});
      const v4u o = {pk(ca[0][0], ca[1][0]), pk(ca[2][0], ca[3][0]), pk(ca[4][0], ca[5][0]), pk(ca[6][0], ca[7][0])};
      *(LAS v4u*)(L + L_QP + (lw * 4 + g) * 272 + r * 16) = o;
    }
    lds_signal(ctl + C_CTX, lane);
    lds_wait(ctl + C_CTX, 4);
    TS(5);
    f32x4 oa[8];
#pragma unroll
    for (int i = 0; i < 8; ++i) oa[i] = f32x4{0.f, 0.f, 0.f, 0.f};
#pragma unroll
    for (int s = 0; s < 4; ++s) {
      const v4u cf = *(const LAS v4u*)(L + L_QP + ((r & 3) * 4 + (r >> 2)) * 272 + ((s + cq) & 3) * 64 + g * 16);
#pragma unroll
      for (int i = 0; i < 8; ++i) oa[i] = mfma16(cf, to[s][i], oa[i]);
    }
    v4u o0, o1;
#pragma unroll
    for (int i = 0; i < 8; ++i) {
      unsigned u = __float_as_uint((oa[i][0] + oa[i][1]) + (oa[i][2] + oa[i][3]));
      u = (u == SENT) ? 0x7FC00000u : u;
      if (i < 4) o0[i] = u; else o1[i - 4] = u;
    }
    const unsigned po = WS_PO + ((p1 * 128 + 32 * g + 8 * lw + (r >> 1)) * 128 + src) * 64 + (r & 1) * 32;
    BST(o0, ws, po);
    BST(o1, ws, po + 16);
    lds_signal(ctl + C_WST, lane);
    TS(6);
  }
  if (lane == 0 && (wave == 0 || wave == 4) && (w == 0 || w == 77 || w == 130 || w == 255))
    printf("wg %d wave %d: %d %d %d %d %d %d %d %d %d %d %d %d %d %d %d\n", w, wave, (int)(tstamp[1]-tstamp[0]), (int)(tstamp[2]-tstamp[0]), (int)(tstamp[3]-tstamp[0]), (int)(tstamp[4]-tstamp[0]), (int)(tstamp[5]-tstamp[0]), (int)(tstamp[6]-tstamp[0]),
      (int)(tstamp[7]-tstamp[0]), (int)(tstamp[8]-tstamp[0]), (int)(tstamp[9]-tstamp[0]), (int)(tstamp[10]-tstamp[0]), (int)(tstamp[11]-tstamp[0]), (int)(tstamp[12]-tstamp[0]), (int)(tstamp[13]-tstamp[0]), (int)(tstamp[14]-tstamp[0]), (int)(tstamp[15]-tstamp[0]));
}

extern "C" void kernel_launch(void* const* d_in, const int* in_sizes, int n_in, void* d_out, int out_size, void* d_ws,
                              size_t ws_size, hipStream_t stream) {
  Params P;
  for (int p = 0; p < 2; ++p) {
    P.x[p] = d_in[6 * p + 0]; P.ckv[p] = d_in[6 * p + 1]; P.wq[p] = d_in[6 * p + 2];
    P.wuk[p] = d_in[6 * p + 3]; P.wuv[p] = d_in[6 * p + 4]; P.wo[p] = d_in[6 * p + 5];
  }
  P.out = d_out; P.ws = d_ws;
  (void)hipFuncSetAttribute((const void*)mla_fused, hipFuncAttributeMaxDynamicSharedMemorySize, (int)L_END);
  mla_fused<<<256, 512, L_END, stream>>>(P);
}
```

```cpp
#include <hip/hip_runtime.h>
#include <hip/hip_bf16.h>

#define GAS __attribute__((address_space(1)))
#define LAS __attribute__((address_space(3)))
typedef unsigned v4u __attribute__((ext_vector_type(4)));
typedef unsigned v2u __attribute__((ext_vector_type(2)));
typedef float f32x2 __attribute__((ext_vector_type(2)));
typedef float f32x4 __attribute__((ext_vector_type(4)));
typedef float f32x16 __attribute__((ext_vector_type(16)));
typedef short bf16x8 __attribute__((ext_vector_type(8)));
typedef short s16x4 __attribute__((ext_vector_type(4)));
#define RLX_WG __ATOMIC_RELAXED, __HIP_MEMORY_SCOPE_WORKGROUP
#define DI __device__ __forceinline__

#define KDEPTH 32
#define KB1 0
#define WSTART 4
#define WDEPTH 8
#define PSLEEP 2
#define APRIO 1
#define XS0 24
#define XS 8
#define PG 12
#define FS 16
#define DS0 24
#define DS 8
#define KWAITQ 1
#define TRES 7000
#define STAMPS 0
#define DELAY_TEST 0

constexpr unsigned WS_F1 = 0;
constexpr unsigned WS_XG = 4096;
constexpr unsigned WS_QG = WS_XG + 64 * 4 * 2048;
constexpr unsigned WS_Q7 = WS_QG + 8 * 32768;
constexpr unsigned WS_F2 = WS_Q7 + 8 * 4096;
constexpr unsigned WS_X1 = WS_F2 + 256 * 1024;
constexpr unsigned WS_OG = WS_X1 + 256 * 2048;
constexpr unsigned WS_ST = WS_OG + 256 * 32768;
constexpr unsigned WS_END = WS_ST + 256 * 8 * 32 * 8;

constexpr unsigned L_T = 0;
constexpr unsigned L_WK = 3 * 32768;
constexpr unsigned L_CT = 131072;
constexpr unsigned L_MX = L_CT + 128;
constexpr unsigned L_QA = L_MX + 512;
constexpr unsigned L_SG = L_QA + 1024;
constexpr unsigned L_RED = L_SG;
constexpr unsigned L_QP = L_SG + 8192;
constexpr unsigned L_P = L_SG + 16384;
constexpr unsigned L_LS = L_SG + 24576;
constexpr unsigned L_RED3 = L_SG + 16384;
constexpr unsigned L_END = L_SG + 28 * 1024;
enum { C_QP = 0, C_QB, C_OD, C_QA, C_F1, C_QS, C_KV0, C_KV1, C_KV2, C_KV3, C_ISS, C_MX, C_P, C_LAT, C_CTX, C_WST, C_ABI,
       C_WK, C_QST, C_X1, C_O3, C_PN, C_QR, C_QS2, C_LS, C_N };

constexpr unsigned SENT = 0xFFFFFFFFu;
constexpr unsigned SPIN_CAP = 1u << 16;
constexpr unsigned LDS_CAP = 1u << 22;
constexpr float QSCALE = 0.08838834764831845f * 1.4426950408889634f;

struct Params {
  const void* x[2]; const void* ckv[2]; const void* wq[2]; const void* wuk[2]; const void* wuv[2]; const void* wo[2];
  void* out; void* ws;
};

DI f32x4 mfma16(v4u a, v4u b, f32x4 c) {
  return __builtin_amdgcn_mfma_f32_16x16x32_bf16(__builtin_bit_cast(bf16x8, a), __builtin_bit_cast(bf16x8, b), c, 0, 0, 0);
}
DI f32x16 mfma32(v4u a, v4u b, f32x16 c) {
  return __builtin_amdgcn_mfma_f32_32x32x16_bf16(__builtin_bit_cast(bf16x8, a), __builtin_bit_cast(bf16x8, b), c, 0, 0, 0);
}
typedef __bf16 bf2_t __attribute__((ext_vector_type(2)));
DI unsigned pk(float lo, float hi) {
  f32x2 f = {lo, hi};
  return __builtin_bit_cast(unsigned, __builtin_convertvector(f, bf2_t));
}
DI unsigned pk_ns(float lo, float hi) {
  const unsigned u = pk(lo, hi);
  return (u == SENT) ? 0x7FC07FC0u : u;
}
DI v4u tcol(const v4u (&w)[8], int i) {
  const unsigned sel = (i & 1) ? 0x07060302u : 0x05040100u;
  v4u o;
  o.x = __builtin_amdgcn_perm(w[1][i >> 1], w[0][i >> 1], sel);
  o.y = __builtin_amdgcn_perm(w[3][i >> 1], w[2][i >> 1], sel);
  o.z = __builtin_amdgcn_perm(w[5][i >> 1], w[4][i >> 1], sel);
  o.w = __builtin_amdgcn_perm(w[7][i >> 1], w[6][i >> 1], sel);
  return o;
}
#define CFENCE() asm volatile("" ::: "memory")
#define SB() __builtin_amdgcn_sched_barrier(0)
DI void lds_signal(LAS unsigned* c, int lane) {
  unsigned long long keep;
  asm volatile("s_mov_b64 %0, exec\n\ts_mov_b64 exec, 1\n\tds_add_u32 %1, %2\n\ts_mov_b64 exec, %0"
               : "=&s"(keep) : "v"((unsigned)(size_t)c), "v"(1u) : "memory");
}
DI unsigned lds_peek(LAS unsigned* c) { return (unsigned)__builtin_amdgcn_readfirstlane(__hip_atomic_load(c, RLX_WG)); }
DI void lds_wait_slow(LAS unsigned* c, unsigned target) {
  CFENCE();
  for (unsigned n = 0; n < LDS_CAP && lds_peek(c) < target; ++n) __builtin_amdgcn_s_sleep(1);
  CFENCE();
}
DI void lds_wait(LAS unsigned* c, unsigned target) {
  CFENCE();
  for (unsigned n = 0; n < LDS_CAP && lds_peek(c) < target; ++n) {}
  CFENCE();
}
DI float xor32_max(float v) {
  auto r = __builtin_amdgcn_permlane32_swap(__float_as_uint(v), __float_as_uint(v), false, false);
  return fmaxf(__uint_as_float(r[0]), __uint_as_float(r[1]));
}
DI float xor32_sum(float v) {
  auto r = __builtin_amdgcn_permlane32_swap(__float_as_uint(v), __float_as_uint(v), false, false);
  return __uint_as_float(r[0]) + __uint_as_float(r[1]);
}
DI float xor16_max(float v) {
  auto r = __builtin_amdgcn_permlane16_swap(__float_as_uint(v), __float_as_uint(v), false, false);
  return fmaxf(__uint_as_float(r[0]), __uint_as_float(r[1]));
}
DI float xor16_sum(float v) {
  auto r = __builtin_amdgcn_permlane16_swap(__float_as_uint(v), __float_as_uint(v), false, false);
  return __uint_as_float(r[0]) + __uint_as_float(r[1]);
}
template <int CTRL> DI float dpp_add(float v) {
  return v + __uint_as_float(__builtin_amdgcn_update_dpp(0u, __float_as_uint(v), CTRL, 0xF, 0xF, false));
}
DI void dma16(const void* sbase, unsigned voff, unsigned lds_dst) {
  unsigned keep;
  asm volatile("s_mov_b32 %0, m0\n\ts_mov_b32 m0, %3\n\ts_nop 0\n\tglobal_load_lds_dwordx4 %1, %2 nt\n\ts_mov_b32 m0, %0"
               : "=&s"(keep) : "v"(voff), "s"(sbase), "s"(lds_dst) : "memory");
}
template <int N> DI void wait_vm() { asm volatile("s_waitcnt vmcnt(%0)" ::"i"(N) : "memory"); }
#define STAMP(i) do {} while (0)
#define TS(i) do {} while (0)
#define TC(i) do {} while (0)
#define RSRC(ptr, bytes) __builtin_amdgcn_make_buffer_rsrc((void*)(ptr), 0, (int)(bytes), 0x00020000)
#define BLD(rs, vo, so, aux) __builtin_amdgcn_raw_buffer_load_b128(rs, (int)(vo), (int)(so), aux)
#define BLD64(rs, vo, so) __builtin_amdgcn_raw_buffer_load_b64(rs, (int)(vo), (int)(so), SC1)
#define BLD32(rs, vo, so) __builtin_amdgcn_raw_buffer_load_b32(rs, (int)(vo), (int)(so), SC1)
#define BST(v, rs, vo) __builtin_amdgcn_raw_buffer_store_b128(v, rs, (int)(vo), 0, SC1)
#define BST64(v, rs, vo) __builtin_amdgcn_raw_buffer_store_b64(v, rs, (int)(vo), 0, SC1)
#define BST32(v, rs, vo) __builtin_amdgcn_raw_buffer_store_b32(v, rs, (int)(vo), 0, SC1)
constexpr int SC1 = 16, NT = 2;

__global__ void __launch_bounds__(512, 2) mla_fused(Params P) {
  extern __shared__ __attribute__((aligned(16))) unsigned char smem[];
  LAS unsigned char* L = (LAS unsigned char*)smem;
  LAS unsigned* ctl = (LAS unsigned*)(L + L_CT);
  const int tid = threadIdx.x, lane = tid & 63;
  const int wave = __builtin_amdgcn_readfirstlane(tid >> 6);
  const int w = blockIdx.x;
  const int ph = w & 63, p1 = ph >> 5, h = w & 31, cq = w >> 6;
  const int src = h * 4 + cq;
  const int blk1 = p1 * 128 + src;
  const int pb = w & 7, p2 = pb >> 2, b2 = pb & 3, j = w >> 3;
  const int r = lane & 15, g = lane >> 4;
  const __amdgpu_buffer_rsrc_t ws = RSRC(P.ws, WS_END);
  TS(0);
  if (tid < C_N) ctl[tid] = 0u;
  asm volatile("s_waitcnt lgkmcnt(0)" ::: "memory");
  __builtin_amdgcn_s_barrier();

  if (wave < 4) {
    const int aw = wave;
    __builtin_amdgcn_s_setprio(APRIO);
    const __amdgpu_buffer_rsrc_t rx = RSRC(P.x[p1], 4 * 512 * 2);
    const __amdgpu_buffer_rsrc_t rq = RSRC(P.wq[p1], 512 * 4096 * 2);
    const unsigned xo = WS_XG + ph * 8192 + tid * 8;
    const unsigned f1own = WS_F1 + ((p1 * 4 + (lane & 3)) * 128 + src) * 4;
    unsigned xtag = BLD32(ws, xo + cq * 2048 + 4, 0);
    unsigned xs[3];
#pragma unroll
    for (int k = 0; k < 3; ++k) xs[k] = BLD32(ws, xo + 4, ((cq + 1 + k) & 3) * 2048);
    v2u f1s = {0u, 0u};
    unsigned f1tag = 0u;
    if (aw == 0) { f1s = BLD64(ws, WS_F1 + pb * 512 + lane * 8, 0); f1tag = BLD32(ws, f1own, 0); }
    SB();
    v4u xf, wq[8];
    const unsigned vq = g * 8 * 8192 + h * 256 + r * 16;
#pragma unroll
    for (int jj = 0; jj < 8; ++jj) wq[jj] = BLD(rq, vq, (128 * cq + 32 * aw + jj) * 8192, NT);
    xf = BLD(rx, (r >> 2) * 1024 + g * 16, (128 * cq + 32 * aw) * 2, 0);
    SB();
    lds_signal(ctl + C_ABI, lane);
    TS(1);
    unsigned xval;
    {
      f32x4 acc[8];
#pragma unroll
      for (int i = 0; i < 8; ++i) acc[i] = mfma16(xf, tcol(wq, i), f32x4{0.f, 0.f, 0.f, 0.f});
      *(LAS f32x4*)(L + L_RED + (aw * 4 + g) * 512 + r * 32) = f32x4{acc[0][0], acc[1][0], acc[2][0], acc[3][0]};
      *(LAS f32x4*)(L + L_RED + (aw * 4 + g) * 512 + r * 32 + 16) = f32x4{acc[4][0], acc[5][0], acc[6][0], acc[7][0]};
      lds_signal(ctl + C_QP, lane);
      lds_wait(ctl + C_QP, 4);
      f32x2 s = *(const LAS f32x2*)(L + L_RED + aw * 512 + lane * 8);
#pragma unroll
      for (int k = 1; k < 4; ++k) s += *(const LAS f32x2*)(L + L_RED + (k * 4 + aw) * 512 + lane * 8);
      xval = pk(s[0], s[1]);
    }
    xtag += 1u;
    BST64((v2u{xval, xtag}), ws, xo + cq * 2048);
    *(LAS unsigned*)(L + L_QP + (cq * 4 + aw) * 272 + lane * 4) = xval;
    TS(2);
#define HEARTBEAT(flags_too) do { xtag += 1u; BST64((v2u{xval, xtag}), ws, xo + cq * 2048); \
      if ((flags_too) && aw == 0) { f1tag += 1u; if (lane < 4) BST32(f1tag, ws, f1own); } } while (0)
    {
      v2u xv[3];
      __builtin_amdgcn_s_sleep(XS0);
      for (unsigned spins = 0; spins < SPIN_CAP; ++spins) {
        CFENCE();
#pragma unroll
        for (int k = 0; k < 3; ++k) xv[k] = BLD64(ws, xo, ((cq + 1 + k) & 3) * 2048);
        if (__all(xv[0].y != xs[0] && xv[1].y != xs[1] && xv[2].y != xs[2])) break;
        if ((spins & 3u) == 3u) HEARTBEAT(false);
        __builtin_amdgcn_s_sleep(XS);
      }
#pragma unroll
      for (int k = 0; k < 3; ++k) *(LAS unsigned*)(L + L_QP + (((cq + 1 + k) & 3) * 4 + aw) * 272 + lane * 4) = xv[k].x;
    }
    lds_signal(ctl + C_QB, lane);
    TS(16);
    {
      lds_wait(ctl + C_WK, 1);
      v4u wk[2][4];
#pragma unroll
      for (int t = 0; t < 2; ++t)
#pragma unroll
        for (int s = 0; s < 4; ++s) wk[t][s] = *(const LAS v4u*)(L + L_WK + ((aw * 2 + t) * 4 + s) * 1024 + lane * 16);
      lds_wait(ctl + C_QB, 4);
      f32x4 a2[2] = {f32x4{0.f, 0.f, 0.f, 0.f}, f32x4{0.f, 0.f, 0.f, 0.f}};
#pragma unroll
      for (int s = 0; s < 4; ++s) {
        const v4u qf = *(const LAS v4u*)(L + L_QP + ((r & 3) * 4 + (r >> 2)) * 272 + s * 64 + g * 16);
        a2[0] = mfma16(qf, wk[0][s], a2[0]);
        a2[1] = mfma16(qf, wk[1][s], a2[1]);
      }
#pragma unroll
      for (int t = 0; t < 2; ++t) {
        const float v = ((a2[t][0] + a2[t][1]) + (a2[t][2] + a2[t][3])) * QSCALE;
        *(LAS unsigned short*)(L + L_QA + g * 256 + (32 * aw + 16 * t + r) * 2) = (unsigned short)(pk(v, v) & 0xFFFFu);
      }
    }
    lds_signal(ctl + C_QA, lane);
    if (aw == 0) {
      lds_wait(ctl + C_QA, 5);
      const v4u v = *(const LAS v4u*)(L + L_QA + lane * 16);
      BST(v, ws, (cq == 3 && r >= 8) ? WS_Q7 + (((p1 * 4 + g) * 4 + ((r - 8) >> 1)) * 64 + (r & 1) * 32 + h) * 16
                                     : WS_QG + ((p1 * 4 + g) * 32 + h) * 1024 + cq * 256 + r * 16);
      lds_signal(ctl + C_QST, lane);
      TS(14);
      wait_vm<0>();
      f1tag += 1u;
      if (lane < 4) BST32(f1tag, ws, f1own);
      TS(3);
    }
    const int r32 = lane & 31, hi = lane >> 5;
    v4u ta[32];
    const unsigned tbase = L_T + (32 * aw + r32) * 256;
    const unsigned tswz = ((r32 & 3) << 2) | ((r32 >> 2) & 3);
#define TA_LOAD(q) do { _Pragma("unroll") for (int s8 = 0; s8 < 8; ++s8) \
      ta[8 * (q) + s8] = *(const LAS v4u*)(L + tbase + (q) * 32768 + (((2 * s8 + hi) ^ tswz) << 4)); SB(); } while (0)
    lds_wait_slow(ctl + C_KV0, 1);
    TA_LOAD(0);
    lds_wait_slow(ctl + C_KV1, 1);
    TA_LOAD(1);
    if (aw == 0) {
      bool c0 = false, c1 = false;
      lds_wait_slow(ctl + C_PN, 1);
      for (unsigned spins = 0; spins < SPIN_CAP; ++spins) {
        CFENCE();
        const v2u f = BLD64(ws, WS_F1 + pb * 512 + lane * 8, 0);
        c0 = c0 || f.x != f1s.x; c1 = c1 || f.y != f1s.y;
        if (__all(c0 && c1)) break;
        if ((spins & 1u) == 1u) HEARTBEAT(true);
        __builtin_amdgcn_s_sleep(FS);
      }
      lds_signal(ctl + C_F1, lane);
    } else {
      CFENCE();
      for (unsigned spins = 0; spins < LDS_CAP && lds_peek(ctl + C_F1) < 1u; ++spins) {
        __builtin_amdgcn_s_sleep(1);
        if ((spins & 127u) == 127u) HEARTBEAT(false);
      }
      CFENCE();
    }
    TS(4);
    const int qrow = 8 * aw + (lane >> 3);
    const unsigned qwr = L_SG + qrow * 128 + (((lane & 7) ^ ((qrow >> 1) & 7)) << 4);
#define QRD(s) (*(const LAS v4u*)(L + L_SG + (((s) >> 2) & 7) % 7 * 4096 + r32 * 128 + (((2 * ((s) & 3) + hi) ^ ((r32 >> 1) & 7)) << 4)))
    v4u q7[4];
    {
      v4u sg[7];
#pragma unroll
      for (int s = 0; s < 7; ++s) sg[s] = BLD(ws, qrow * 1024 + (lane & 7) * 16 + s * 128, WS_QG + pb * 32768, SC1);
#pragma unroll
      for (int s = 0; s < 4; ++s) q7[s] = BLD(ws, lane * 16 + s * 1024, WS_Q7 + pb * 4096, SC1);
      SB();
      lds_wait_slow(ctl + C_KV2, 1);
      TA_LOAD(2);
#pragma unroll
      for (int s = 0; s < 7; ++s) *(LAS v4u*)(L + qwr + s * 4096) = sg[s];
      lds_signal(ctl + C_QS, lane);
    }
    TS(5);
    lds_wait(ctl + C_KV3, 1);
    TA_LOAD(3);
    TS(6);
    lds_wait(ctl + C_QS, 4);
    TS(17);
    s16x4 tf[8][4][2];
    const int q4 = (lane & 15) >> 2, pp = lane & 3, blk = (lane >> 4) & 1;
#define TRADDR(ks, ct, t) (L_T + aw * 32768 + (ks) * 4096 + (8 * (t) + 4 * hi + q4) * 256 + (((ct) ^ q4) << 6) + \
                           (((2 * blk + (pp >> 1)) ^ ((2 * (t) + hi) & 3)) << 4) + ((pp & 1) << 3))
    f32x16 sa;
#pragma unroll
    for (int i = 0; i < 16; ++i) sa[i] = 0.f;
    {
      constexpr int PD = 4;
      v4u qf[PD];
#pragma unroll
      for (int s = 0; s < PD; ++s) qf[s] = QRD(s);
      SB();
#pragma unroll
      for (int s = 0; s < 32; ++s) {
        sa = mfma32(ta[s], s < 28 ? qf[s % PD] : q7[s & 3], sa);
        if (s + PD < 28) qf[s % PD] = QRD(s + PD);
        if (s >= 1) {
          const int n = s - 1;
          const int ct = (n >> 4) * 2 + (n & 1), ks = (n >> 1) & 7;
          tf[ks][ct][0] = __builtin_amdgcn_ds_read_tr16_b64_v4i16((LAS s16x4*)(L + TRADDR(ks, ct, 0)));
          tf[ks][ct][1] = __builtin_amdgcn_ds_read_tr16_b64_v4i16((LAS s16x4*)(L + TRADDR(ks, ct, 1)));
        }
        SB();
      }
      tf[7][3][0] = __builtin_amdgcn_ds_read_tr16_b64_v4i16((LAS s16x4*)(L + TRADDR(7, 3, 0)));
      tf[7][3][1] = __builtin_amdgcn_ds_read_tr16_b64_v4i16((LAS s16x4*)(L + TRADDR(7, 3, 1)));
    }
    TC(22);
    float mx = sa[0];
#pragma unroll
    for (int i = 1; i < 16; ++i) mx = fmaxf(mx, sa[i]);
    mx = xor32_max(mx);
    if (hi == 0) *(LAS float*)(L + L_MX + r32 * 16 + aw * 4) = mx;
    lds_signal(ctl + C_MX, lane);
    TC(23);
    lds_wait(ctl + C_MX, 4);
    TC(24);
    {
      const f32x4 m4 = *(const LAS f32x4*)(L + L_MX + r32 * 16);
      mx = fmaxf(fmaxf(m4[0], m4[1]), fmaxf(m4[2], m4[3]));
    }
    {
#pragma unroll
      for (int i = 0; i < 16; ++i) sa[i] = __builtin_amdgcn_exp2f(sa[i] - mx);
      const v4u p0 = {pk(sa[0], sa[1]), pk(sa[2], sa[3]), pk(sa[4], sa[5]), pk(sa[6], sa[7])};
      const v4u p1v = {pk(sa[8], sa[9]), pk(sa[10], sa[11]), pk(sa[12], sa[13]), pk(sa[14], sa[15])};
      *(LAS v4u*)(L + L_P + (2 * aw) * 1024 + lane * 16) = p0;
      *(LAS v4u*)(L + L_P + (2 * aw + 1) * 1024 + lane * 16) = p1v;
      lds_signal(ctl + C_P, lane);
      SB();
      float ps = (((sa[0] + sa[1]) + (sa[2] + sa[3])) + ((sa[4] + sa[5]) + (sa[6] + sa[7]))) +
                 (((sa[8] + sa[9]) + (sa[10] + sa[11])) + ((sa[12] + sa[13]) + (sa[14] + sa[15])));
      ps = xor32_sum(ps);
      if (hi == 0) *(LAS float*)(L + L_LS + r32 * 16 + aw * 4) = ps;
      lds_signal(ctl + C_LS, lane);
    }
    TC(25);
    lds_wait(ctl + C_P, 4);
    TC(26);
    TS(7);
    v4u pf[8];
#pragma unroll
    for (int ks = 0; ks < 8; ++ks) pf[ks] = *(const LAS v4u*)(L + L_P + ks * 1024 + lane * 16);
    f32x16 oa[4];
#pragma unroll
    for (int ct = 0; ct < 4; ++ct)
#pragma unroll
      for (int i = 0; i < 16; ++i) oa[ct][i] = 0.f;
#define PV_MFMA(ks, ct) oa[ct] = mfma32(__builtin_bit_cast(v4u, __builtin_shufflevector(tf[ks][ct][0], tf[ks][ct][1], 0, 1, 2, 3, 4, 5, 6, 7)), pf[ks], oa[ct])
#define O_WRITE(half, ct, gg) *(LAS v2u*)(L + L_T + aw * 32768 + (half) * 4096 + r32 * 128 + (((4 * ((ct) & 1) + (gg)) ^ (r32 & 7)) << 4) + hi * 8) = \
      v2u{pk(oa[ct][4 * (gg)], oa[ct][4 * (gg) + 1]), pk(oa[ct][4 * (gg) + 2], oa[ct][4 * (gg) + 3])}
    const unsigned ord = L_T + aw * 32768 + (lane >> 3) * 128 + (((lane & 7) ^ ((lane >> 3) & 7)) << 4);
    const unsigned ost = WS_OG + ((p2 * 128 + (lane >> 3) * 4 + aw) * 128 + b2 * 32 + j) * 256 + (lane & 7) * 16;
#pragma unroll
    for (int ks = 0; ks < 8; ++ks) { PV_MFMA(ks, 0); PV_MFMA(ks, 1); }
    SB();
    {
      v4u ov[4];
#pragma unroll
      for (int ks = 0; ks < 8; ++ks) {
        PV_MFMA(ks, 2);
        if (ks >= 1 && ks <= 4) { O_WRITE(0, 0, ks - 1); }
        SB();
        PV_MFMA(ks, 3);
        if (ks >= 1 && ks <= 4) { O_WRITE(0, 1, ks - 1); }
        if (ks == 5) {
          CFENCE();
#pragma unroll
          for (int u = 0; u < 4; ++u) ov[u] = *(const LAS v4u*)(L + ord + u * 1024);
        }
        if (ks == 7) {
#pragma unroll
          for (int u = 0; u < 4; ++u) __builtin_amdgcn_raw_buffer_store_b128(ov[u], ws, (int)ost, u * 32 * 32768, SC1);
        }
        SB();
      }
    }
    TC(27);
    TS(8);
#pragma unroll
    for (int gg = 0; gg < 4; ++gg) { O_WRITE(1, 2, gg); O_WRITE(1, 3, gg); }
    CFENCE();
    {
      v4u ov[4];
#pragma unroll
      for (int u = 0; u < 4; ++u) ov[u] = *(const LAS v4u*)(L + ord + 4096 + u * 1024);
#pragma unroll
      for (int u = 0; u < 4; ++u) __builtin_amdgcn_raw_buffer_store_b128(ov[u], ws, (int)(ost + 128), u * 32 * 32768, SC1);
    }
    lds_wait(ctl + C_LS, 4);
    const f32x4 l4 = *(const LAS f32x4*)(L + L_LS + r32 * 16);
    const float lsum = (l4[0] + l4[1]) + (l4[2] + l4[3]);
    TS(15);
    TC(29);
    wait_vm<0>();
    if (hi == 0) {
      const v2u gr = {__float_as_uint(mx), __float_as_uint(lsum) | 1u};
      BST64(gr, ws, WS_F2 + ((p2 * 128 + r32 * 4 + aw) * 128 + b2 * 32 + j) * 8);
    }
    TC(30);
    lds_signal(ctl + C_OD, lane);
    TS(9);
    {
      v4u st[4];
      __builtin_amdgcn_s_sleep(DS0);
      const unsigned fo = WS_F2 + blk1 * 1024 + aw * 256 + g * 64;
      for (unsigned spins = 0; spins < SPIN_CAP; ++spins) {
        CFENCE();
#pragma unroll
        for (int k = 0; k < 4; ++k) st[k] = BLD(ws, fo + k * 16, 0, SC1);
        bool ok = true;
#pragma unroll
        for (int k = 0; k < 4; ++k) ok = ok && (st[k].y != 0u) && (st[k].w != 0u);
        if (__all(ok)) break;
        if ((spins & 7u) == 7u) HEARTBEAT(true);
        __builtin_amdgcn_s_sleep(DS);
      }
      CFENCE();
      TS(10);
      v4u od[8];
#pragma unroll
      for (int i = 0; i < 8; ++i) od[i] = BLD(ws, (8 * g + i) * 256 + r * 16, WS_OG + blk1 * 32768 + aw * 8192, SC1);
      SB();
      float m[8], l[8];
#pragma unroll
      for (int k = 0; k < 4; ++k) {
        m[2 * k] = __uint_as_float(st[k].x); l[2 * k] = __uint_as_float(st[k].y);
        m[2 * k + 1] = __uint_as_float(st[k].z); l[2 * k + 1] = __uint_as_float(st[k].w);
      }
      float M = m[0];
#pragma unroll
      for (int i = 1; i < 8; ++i) M = fmaxf(M, m[i]);
      M = xor32_max(xor16_max(M));
      float Ls = 0.f;
#pragma unroll
      for (int i = 0; i < 8; ++i) { m[i] = __builtin_amdgcn_exp2f(m[i] - M); Ls += m[i] * l[i]; }
      Ls = xor32_sum(xor16_sum(Ls));
      const float inv = 1.0f / Ls;
      v4u wf;
#pragma unroll
      for (int k = 0; k < 4; ++k) {
        const float w0 = m[2 * k] * inv, w1 = m[2 * k + 1] * inv;
        const unsigned hi2 = pk(w0, w1);
        const unsigned lo2 = pk(w0 - __uint_as_float(hi2 << 16), w1 - __uint_as_float(hi2 & 0xFFFF0000u));
        wf[k] = (r & 1) ? lo2 : hi2;
      }
      SB();
      f32x4 la[8];
#pragma unroll
      for (int e = 0; e < 8; ++e) la[e] = mfma16(wf, tcol(od, e), f32x4{0.f, 0.f, 0.f, 0.f});
      const v4u o = {pk(la[0][0] + la[0][1], la[1][0] + la[1][1]), pk(la[2][0] + la[2][1], la[3][0] + la[3][1]),
                     pk(la[4][0] + la[4][1], la[5][0] + la[5][1]), pk(la[6][0] + la[6][1], la[7][0] + la[7][1])};
      if (g == 0) *(LAS v4u*)(L + L_QA + aw * 256 + r * 16) = o;
      lds_signal(ctl + C_LAT, lane);
      TS(11);
    }
    if (aw == 0) {
      const int eb = h & 3, em = 4 * ((h >> 2) & 1) + (h >> 3);
      lds_wait_slow(ctl + C_WST, 4);
      v4u pv;
      const unsigned po = WS_X1 + (p1 * 128 + (lane >> 1) * 4 + cq) * 2048 + 1024 + eb * 256 + em * 32 + (lane & 1) * 16;
      for (unsigned spins = 0; spins < SPIN_CAP; ++spins) {
        CFENCE();
        pv = BLD(ws, po, 0, SC1);
        if (__all(pv.x != SENT && pv.y != SENT && pv.z != SENT && pv.w != SENT)) break;
        __builtin_amdgcn_s_sleep(1);
      }
      TS(12);
      float s[8];
#pragma unroll
      for (int c = 0; c < 4; ++c) { s[2 * c] = __uint_as_float(pv[c] << 16); s[2 * c + 1] = __uint_as_float(pv[c] & 0xFFFF0000u); }
#pragma unroll
      for (int c = 0; c < 8; ++c) {
        float v = s[c];
        v = dpp_add<0x4E>(v);
        v = dpp_add<0x124>(v);
        v = dpp_add<0x128>(v);
        v = xor32_sum(xor16_sum(v));
        s[c] = v;
      }
      if (lane < 2) {
        const v4u o = {pk(s[0], s[1]), pk(s[2], s[3]), pk(s[4], s[5]), pk(s[6], s[7])};
        *(GAS v4u*)((char*)P.out + (eb * 1024 + p1 * 512 + cq * 128 + em * 16 + lane * 8) * 2) = o;
      }
      TS(13);
    }
  } else {
    const int lw = wave - 4;
    if (lw == 0) {
      const unsigned lds0 = (unsigned)(size_t)L;
      const unsigned long long tk0 = __builtin_amdgcn_s_memtime();
      __builtin_amdgcn_s_setprio(3);
      if (KWAITQ) lds_wait(ctl + C_QP, 1); else lds_wait(ctl + C_ABI, 4);
      {
        const char* wkb = (const char*)P.wuk[p1] + (size_t)(128 * cq) * 8192 + h * 256;
        const unsigned vk = r * 8192 + g * 16;
        for (int G = 0; G < 4; ++G) {
          wait_vm<KDEPTH - 8>();
#pragma unroll
          for (int i = 0; i < 8; ++i) dma16(wkb + (size_t)(G * 2 + (i >> 2)) * 16 * 8192 + (i & 3) * 64, vk, lds0 + L_WK + (G * 8 + i) * 1024);
        }
      }
      const char* kvb = (const char*)P.ckv[p2] + ((size_t)b2 * 4096 + 128 * j) * 1024;
      unsigned vo[4];
#pragma unroll
      for (int u3 = 0; u3 < 4; ++u3) vo[u3] = g * 1024 + ((r ^ ((g << 2) | u3)) << 4);
      for (int G = 0; G < 16; ++G) {
        if (8 * G == KB1) {
          if (KB1 <= KDEPTH - 8) { wait_vm<(KB1 < 63 ? KB1 : 63)>(); lds_signal(ctl + C_WK, lane); }
          while (lds_peek(ctl + C_QST) < 1u && __builtin_amdgcn_s_memtime() - tk0 < (unsigned long long)TRES) __builtin_amdgcn_s_sleep(1);
          CFENCE();
        }
        wait_vm<KDEPTH - 8>();
        const int done = 8 * G - (KDEPTH - 8);
        if (done == 0 && !(KB1 <= KDEPTH - 8)) lds_signal(ctl + C_WK, lane);
        if (G == 12) lds_wait_slow(ctl + C_QST, 1);
        if (G == PG) lds_signal(ctl + C_PN, lane);
        if (done == 32) lds_signal(ctl + C_KV0, lane);
        if (done == 64) lds_signal(ctl + C_KV1, lane);
        if (done == 96) lds_signal(ctl + C_KV2, lane);
        const int q = G >> 2, u0 = (G & 3) * 8;
        const char* gb = kvb + u0 * 4096 + q * 256;
        const unsigned lb = lds0 + L_T + q * 32768 + u0 * 1024;
#pragma unroll
        for (int i = 0; i < 8; ++i) dma16(gb + i * 4096, vo[i & 3], lb + i * 1024);
      }
      lds_signal(ctl + C_ISS, lane);
      if (PG >= 16) lds_signal(ctl + C_PN, lane);
      TS(1);
      if (120 - (KDEPTH - 8) < 96) { wait_vm<32>(); lds_signal(ctl + C_KV2, lane); }
      wait_vm<0>();
      lds_signal(ctl + C_KV3, lane);
      __builtin_amdgcn_s_setprio(0);
      TS(2);
    } else {
      if (lw == 3) {
        const v4u z = {0u, 0u, 0u, 0u}, sn = {SENT, SENT, SENT, SENT};
        BST(z, ws, WS_F2 + blk1 * 1024 + lane * 16);
        BST(sn, ws, WS_X1 + blk1 * 2048 + lane * 16);
        BST(sn, ws, WS_X1 + blk1 * 2048 + 1024 + lane * 16);
        wait_vm<0>();
        lds_signal(ctl + C_QA, lane);
      }
      if (WSTART == 0) lds_wait_slow(ctl + C_ISS, 1);
      if (WSTART == 1) lds_wait_slow(ctl + C_KV3, 1);
      if (WSTART >= 3) lds_wait_slow(ctl + C_ISS, 1);
    }
    if (WSTART == 2) lds_wait_slow(ctl + C_OD, 4);
    if (WSTART == 3 || WSTART == 4) lds_wait_slow(ctl + C_QS, 4);
    const __amdgpu_buffer_rsrc_t rv = RSRC(P.wuv[p1], 512 * 4096 * 2);
    const __amdgpu_buffer_rsrc_t ro = RSRC(P.wo[p1], 512 * 4096 * 2);
    v4u wv[8], wo[8];
#pragma unroll
    for (int jj = 0; jj < 8; ++jj) wv[jj] = BLD(rv, g * 8 * 8192 + h * 256 + r * 16, (128 * cq + 32 * lw + jj) * 8192, NT);
    SB(); if (WDEPTH < 8) wait_vm<WDEPTH < 63 ? WDEPTH : 63>();
    if (WSTART == 4) lds_wait_slow(ctl + C_OD, 4);
    if (WSTART == 5) lds_wait_slow(ctl + C_QS, 4);
#pragma unroll
    for (int jj = 0; jj < 8; ++jj) wo[jj] = BLD(ro, g * 8 * 1024 + r * 16, (h * 128 + 32 * lw + jj) * 1024 + cq * 256, NT);
    SB();
    TS(3);
    v4u tv[8], to[8];
#pragma unroll
    for (int i = 0; i < 8; ++i) { tv[i] = tcol(wv, i); asm volatile("" : "+v"(tv[i])); }
#pragma unroll
    for (int i = 0; i < 8; ++i) { to[i] = tcol(wo, i); asm volatile("" : "+v"(to[i])); }
    TS(4);
    lds_wait_slow(ctl + C_LAT, 4);
    {
      const v4u lf = *(const LAS v4u*)(L + L_QA + (r >> 2) * 256 + lw * 64 + g * 16);
      f32x4 ca[8];
#pragma unroll
      for (int i = 0; i < 8; ++i) ca[i] = mfma16(lf, tv[i], f32x4{0.f, 0.f, 0.f, 0.f});
      *(LAS f32x4*)(L + L_RED + (lw * 4 + g) * 512 + r * 32) = f32x4{ca[0][0], ca[1][0], ca[2][0], ca[3][0]};
      *(LAS f32x4*)(L + L_RED + (lw * 4 + g) * 512 + r * 32 + 16) = f32x4{ca[4][0], ca[5][0], ca[6][0], ca[7][0]};
      lds_signal(ctl + C_CTX, lane);
      lds_wait(ctl + C_CTX, 4);
      f32x2 s = *(const LAS f32x2*)(L + L_RED + lw * 512 + lane * 8);
#pragma unroll
      for (int k = 1; k < 4; ++k) s += *(const LAS f32x2*)(L + L_RED + (k * 4 + lw) * 512 + lane * 8);
      const unsigned cv = pk_ns(s[0], s[1]);
      BST32(cv, ws, WS_X1 + blk1 * 2048 + lw * 256 + lane * 4);
      *(LAS unsigned*)(L + L_QP + (cq * 4 + lw) * 272 + lane * 4) = cv;
    }
    TS(5);
    {
      unsigned xv[3];
      const unsigned xb = WS_X1 + (p1 * 128 + h * 4) * 2048 + lw * 256 + lane * 4;
      for (unsigned spins = 0; spins < SPIN_CAP; ++spins) {
        CFENCE();
#pragma unroll
        for (int k = 0; k < 3; ++k) xv[k] = BLD32(ws, xb, ((cq + 1 + k) & 3) * 2048);
        if (__all(xv[0] != SENT && xv[1] != SENT && xv[2] != SENT)) break;
        __builtin_amdgcn_s_sleep(1);
      }
#pragma unroll
      for (int k = 0; k < 3; ++k) *(LAS unsigned*)(L + L_QP + (((cq + 1 + k) & 3) * 4 + lw) * 272 + lane * 4) = xv[k];
    }
    lds_signal(ctl + C_X1, lane);
    TS(7);
    lds_wait(ctl + C_X1, 4);
    {
      const v4u cf = *(const LAS v4u*)(L + L_QP + ((r & 3) * 4 + (r >> 2)) * 272 + lw * 64 + g * 16);
      float o[8];
#pragma unroll
      for (int i = 0; i < 8; ++i) {
        const f32x4 a = mfma16(cf, to[i], f32x4{0.f, 0.f, 0.f, 0.f});
        o[i] = (a[0] + a[1]) + (a[2] + a[3]);
      }
      *(LAS f32x4*)(L + L_RED3 + (lw * 4 + g) * 512 + r * 32) = f32x4{o[0], o[1], o[2], o[3]};
      *(LAS f32x4*)(L + L_RED3 + (lw * 4 + g) * 512 + r * 32 + 16) = f32x4{o[4], o[5], o[6], o[7]};
      lds_signal(ctl + C_O3, lane);
      lds_wait(ctl + C_O3, 4);
      f32x2 s = *(const LAS f32x2*)(L + L_RED3 + lw * 512 + lane * 8);
#pragma unroll
      for (int k = 1; k < 4; ++k) s += *(const LAS f32x2*)(L + L_RED3 + (k * 4 + lw) * 512 + lane * 8);
      BST32(pk_ns(s[0], s[1]), ws, WS_X1 + blk1 * 2048 + 1024 + lw * 256 + lane * 4);
    }
    lds_signal(ctl + C_WST, lane);
    TS(6);
  }
}

extern "C" void kernel_launch(void* const* d_in, const int* in_sizes, int n_in, void* d_out, int out_size, void* d_ws,
                              size_t ws_size, hipStream_t stream) {
  Params P;
  for (int p = 0; p < 2; ++p) {
    P.x[p] = d_in[6 * p + 0]; P.ckv[p] = d_in[6 * p + 1]; P.wq[p] = d_in[6 * p + 2];
    P.wuk[p] = d_in[6 * p + 3]; P.wuv[p] = d_in[6 * p + 4]; P.wo[p] = d_in[6 * p + 5];
  }
  P.out = d_out; P.ws = d_ws;
  (void)hipFuncSetAttribute((const void*)mla_fused, hipFuncAttributeMaxDynamicSharedMemorySize, (int)L_END);
  mla_fused<<<256, 512, L_END, stream>>>(P);
}
```

```cpp
#include <hip/hip_runtime.h>
#include <hip/hip_bf16.h>

#define GAS __attribute__((address_space(1)))
#define LAS __attribute__((address_space(3)))
typedef unsigned v4u __attribute__((ext_vector_type(4)));
typedef unsigned v2u __attribute__((ext_vector_type(2)));
typedef float f32x2 __attribute__((ext_vector_type(2)));
typedef float f32x4 __attribute__((ext_vector_type(4)));
typedef float f32x16 __attribute__((ext_vector_type(16)));
typedef short bf16x8 __attribute__((ext_vector_type(8)));
typedef short s16x4 __attribute__((ext_vector_type(4)));
#define RLX_WG __ATOMIC_RELAXED, __HIP_MEMORY_SCOPE_WORKGROUP
#define DI __device__ __forceinline__

#define KDEPTH 40
#define PG 12
#define XS0 24
#define XS 8
#define FS 16
#define DS0 16
#define DS 6
#define XPRIO 1
#define TRES 7000
#define WISS 1
#define STAMPS 0
#define DELAY_TEST 0

constexpr unsigned WS_F1 = 0;
constexpr unsigned WS_XG = 4096;
constexpr unsigned WS_QG = WS_XG + 64 * 4 * 2048;
constexpr unsigned WS_Q7 = WS_QG + 8 * 32768;
constexpr unsigned WS_F2 = WS_Q7 + 8 * 4096;
constexpr unsigned WS_X1 = WS_F2 + 256 * 1024;
constexpr unsigned WS_OG = WS_X1 + 256 * 2048;
constexpr unsigned WS_ST = WS_OG + 256 * 32768;
constexpr unsigned WS_END = WS_ST + 256 * 8 * 32 * 8;

constexpr unsigned L_T = 0;
constexpr unsigned L_WK = 3 * 32768;
constexpr unsigned L_CT = 131072;
constexpr unsigned L_MX = L_CT + 128;
constexpr unsigned L_QA = L_MX + 512;
constexpr unsigned L_SG = L_QA + 1024;
constexpr unsigned L_RED = L_SG;
constexpr unsigned L_QP = L_SG + 8192;
constexpr unsigned L_P = L_SG + 16384;
constexpr unsigned L_RED3 = L_SG + 16384;
constexpr unsigned L_LS = L_SG + 28 * 1024;
constexpr unsigned L_END = L_LS + 512;
enum { C_QP = 0, C_QB, C_QA, C_F1, C_QS, C_KV0, C_KV1, C_KV2, C_KV3, C_MX, C_P, C_LS,
       C_LAT, C_CTX, C_WST, C_WK, C_QST, C_X1, C_O3, C_PN, C_OL, C_N };

constexpr unsigned SENT = 0xFFFFFFFFu;
constexpr unsigned SPIN_CAP = 1u << 16;
constexpr unsigned LDS_CAP = 1u << 22;
constexpr float QSCALE = 0.08838834764831845f * 1.4426950408889634f;

struct Params {
  const void* x[2]; const void* ckv[2]; const void* wq[2]; const void* wuk[2]; const void* wuv[2]; const void* wo[2];
  void* out; void* ws;
};

DI f32x4 mfma16(v4u a, v4u b, f32x4 c) {
  return __builtin_amdgcn_mfma_f32_16x16x32_bf16(__builtin_bit_cast(bf16x8, a), __builtin_bit_cast(bf16x8, b), c, 0, 0, 0);
}
DI f32x16 mfma32(v4u a, v4u b, f32x16 c) {
  return __builtin_amdgcn_mfma_f32_32x32x16_bf16(__builtin_bit_cast(bf16x8, a), __builtin_bit_cast(bf16x8, b), c, 0, 0, 0);
}
typedef __bf16 bf2_t __attribute__((ext_vector_type(2)));
DI unsigned pk(float lo, float hi) {
  f32x2 f = {lo, hi};
  return __builtin_bit_cast(unsigned, __builtin_convertvector(f, bf2_t));
}
DI unsigned pk_ns(float lo, float hi) {
  const unsigned u = pk(lo, hi);
  return (u == SENT) ? 0x7FC07FC0u : u;
}
DI v4u tcol(const v4u (&w)[8], int i) {
  const unsigned sel = (i & 1) ? 0x07060302u : 0x05040100u;
  v4u o;
  o.x = __builtin_amdgcn_perm(w[1][i >> 1], w[0][i >> 1], sel);
  o.y = __builtin_amdgcn_perm(w[3][i >> 1], w[2][i >> 1], sel);
  o.z = __builtin_amdgcn_perm(w[5][i >> 1], w[4][i >> 1], sel);
  o.w = __builtin_amdgcn_perm(w[7][i >> 1], w[6][i >> 1], sel);
  return o;
}
#define CFENCE() asm volatile("" ::: "memory")
#define SB() __builtin_amdgcn_sched_barrier(0)
DI void lds_signal(LAS unsigned* c) {
  unsigned long long keep;
  asm volatile("s_mov_b64 %0, exec\n\ts_mov_b64 exec, 1\n\tds_add_u32 %1, %2\n\ts_mov_b64 exec, %0"
               : "=&s"(keep) : "v"((unsigned)(size_t)c), "v"(1u) : "memory");
}
DI unsigned lds_peek(LAS unsigned* c) { return (unsigned)__builtin_amdgcn_readfirstlane(__hip_atomic_load(c, RLX_WG)); }
DI void lds_wait_slow(LAS unsigned* c, unsigned target) {
  CFENCE();
  for (unsigned n = 0; n < LDS_CAP && lds_peek(c) < target; ++n) __builtin_amdgcn_s_sleep(1);
  CFENCE();
}
DI void lds_wait(LAS unsigned* c, unsigned target) {
  CFENCE();
  for (unsigned n = 0; n < LDS_CAP && lds_peek(c) < target; ++n) {}
  CFENCE();
}
DI float xor32_max(float v) {
  auto r = __builtin_amdgcn_permlane32_swap(__float_as_uint(v), __float_as_uint(v), false, false);
  return fmaxf(__uint_as_float(r[0]), __uint_as_float(r[1]));
}
DI float xor32_sum(float v) {
  auto r = __builtin_amdgcn_permlane32_swap(__float_as_uint(v), __float_as_uint(v), false, false);
  return __uint_as_float(r[0]) + __uint_as_float(r[1]);
}
DI float xor16_max(float v) {
  auto r = __builtin_amdgcn_permlane16_swap(__float_as_uint(v), __float_as_uint(v), false, false);
  return fmaxf(__uint_as_float(r[0]), __uint_as_float(r[1]));
}
DI float xor16_sum(float v) {
  auto r = __builtin_amdgcn_permlane16_swap(__float_as_uint(v), __float_as_uint(v), false, false);
  return __uint_as_float(r[0]) + __uint_as_float(r[1]);
}
template <int CTRL> DI float dpp_add(float v) {
  return v + __uint_as_float(__builtin_amdgcn_update_dpp(0u, __float_as_uint(v), CTRL, 0xF, 0xF, false));
}
DI void dma16(const void* sbase, unsigned voff, unsigned lds_dst) {
  unsigned keep;
  asm volatile("s_mov_b32 %0, m0\n\ts_mov_b32 m0, %3\n\ts_nop 0\n\tglobal_load_lds_dwordx4 %1, %2 nt\n\ts_mov_b32 m0, %0"
               : "=&s"(keep) : "v"(voff), "s"(sbase), "s"(lds_dst) : "memory");
}
template <int N> DI void wait_vm() { asm volatile("s_waitcnt vmcnt(%0)" ::"i"(N) : "memory"); }
#define STAMP(i) do {} while (0)
#define TS(i) do {} while (0)
#define TC(i) do {} while (0)
#define RSRC(ptr, bytes) __builtin_amdgcn_make_buffer_rsrc((void*)(ptr), 0, (int)(bytes), 0x00020000)
#define BLD(rs, vo, so, aux) __builtin_amdgcn_raw_buffer_load_b128(rs, (int)(vo), (int)(so), aux)
#define BLD64(rs, vo, so) __builtin_amdgcn_raw_buffer_load_b64(rs, (int)(vo), (int)(so), SC1)
#define BLD32(rs, vo, so) __builtin_amdgcn_raw_buffer_load_b32(rs, (int)(vo), (int)(so), SC1)
#define BST(v, rs, vo) __builtin_amdgcn_raw_buffer_store_b128(v, rs, (int)(vo), 0, SC1)
#define BST64(v, rs, vo) __builtin_amdgcn_raw_buffer_store_b64(v, rs, (int)(vo), 0, SC1)
#define BST32(v, rs, vo) __builtin_amdgcn_raw_buffer_store_b32(v, rs, (int)(vo), 0, SC1)
constexpr int SC1 = 16, NT = 2;

__global__ void __launch_bounds__(512, 2) mla_fused(Params P) {
  extern __shared__ __attribute__((aligned(16))) unsigned char smem[];
  LAS unsigned char* L = (LAS unsigned char*)smem;
  LAS unsigned* ctl = (LAS unsigned*)(L + L_CT);
  const int tid = threadIdx.x, lane = tid & 63;
  const int wave = __builtin_amdgcn_readfirstlane(tid >> 6);
  const int w = blockIdx.x;
  const int ph = w & 63, p1 = ph >> 5, h = w & 31, cq = w >> 6;
  const int src = h * 4 + cq;
  const int blk1 = p1 * 128 + src;
  const int pb = w & 7, p2 = pb >> 2, b2 = pb & 3, j = w >> 3;
  const unsigned oblk = p2 * 128 * 128 + b2 * 32 + j;
  const int r = lane & 15, g = lane >> 4;
  const __amdgpu_buffer_rsrc_t ws = RSRC(P.ws, WS_END);
  TS(0);
  if (tid < C_N) ctl[tid] = 0u;
  asm volatile("s_waitcnt lgkmcnt(0)" ::: "memory");
  __builtin_amdgcn_s_barrier();

  if (wave < 4) {
    const int aw = wave;
    __builtin_amdgcn_s_setprio(XPRIO);
    const __amdgpu_buffer_rsrc_t rx = RSRC(P.x[p1], 4 * 512 * 2);
    const __amdgpu_buffer_rsrc_t rq = RSRC(P.wq[p1], 512 * 4096 * 2);
    const unsigned xo = WS_XG + ph * 8192 + tid * 8;
    const unsigned f1own = WS_F1 + ((p1 * 4 + (lane & 3)) * 128 + src) * 4;
    unsigned xtag = BLD32(ws, xo + cq * 2048 + 4, 0);
    unsigned xs[3];
#pragma unroll
    for (int k = 0; k < 3; ++k) xs[k] = BLD32(ws, xo + 4, ((cq + 1 + k) & 3) * 2048);
    v2u f1s = {0u, 0u};
    unsigned f1tag = 0u;
    if (aw == 0) { f1s = BLD64(ws, WS_F1 + pb * 512 + lane * 8, 0); f1tag = BLD32(ws, f1own, 0); }
    SB();
    v4u xf, wq[8];
#pragma unroll
    for (int jj = 0; jj < 8; ++jj) wq[jj] = BLD(rq, g * 8 * 8192 + h * 256 + r * 16, (128 * cq + 32 * aw + jj) * 8192, NT);
    xf = BLD(rx, (r >> 2) * 1024 + g * 16, (128 * cq + 32 * aw) * 2, 0);
    SB();
    TS(1);
    unsigned xval;
    {
      f32x4 acc[8];
#pragma unroll
      for (int i = 0; i < 8; ++i) acc[i] = mfma16(xf, tcol(wq, i), f32x4{0.f, 0.f, 0.f, 0.f});
      *(LAS f32x4*)(L + L_RED + (aw * 4 + g) * 512 + r * 32) = f32x4{acc[0][0], acc[1][0], acc[2][0], acc[3][0]};
      *(LAS f32x4*)(L + L_RED + (aw * 4 + g) * 512 + r * 32 + 16) = f32x4{acc[4][0], acc[5][0], acc[6][0], acc[7][0]};
      lds_signal(ctl + C_QP);
      lds_wait(ctl + C_QP, 4);
      f32x2 s = *(const LAS f32x2*)(L + L_RED + aw * 512 + lane * 8);
#pragma unroll
      for (int k = 1; k < 4; ++k) s += *(const LAS f32x2*)(L + L_RED + (k * 4 + aw) * 512 + lane * 8);
      xval = pk(s[0], s[1]);
    }
    xtag += 1u;
    BST64((v2u{xval, xtag}), ws, xo + cq * 2048);
    *(LAS unsigned*)(L + L_QP + (cq * 4 + aw) * 272 + lane * 4) = xval;
    TS(2);
#define HEARTBEAT(flags_too) do { xtag += 1u; BST64((v2u{xval, xtag}), ws, xo + cq * 2048); \
      if ((flags_too) && aw == 0) { f1tag += 1u; if (lane < 4) BST32(f1tag, ws, f1own); } } while (0)
    {
      v2u xv[3];
      __builtin_amdgcn_s_sleep(XS0);
      for (unsigned spins = 0; spins < SPIN_CAP; ++spins) {
        CFENCE();
#pragma unroll
        for (int k = 0; k < 3; ++k) xv[k] = BLD64(ws, xo, ((cq + 1 + k) & 3) * 2048);
        if (__all(xv[0].y != xs[0] && xv[1].y != xs[1] && xv[2].y != xs[2])) break;
        if ((spins & 3u) == 3u) HEARTBEAT(false);
        __builtin_amdgcn_s_sleep(XS);
      }
#pragma unroll
      for (int k = 0; k < 3; ++k) *(LAS unsigned*)(L + L_QP + (((cq + 1 + k) & 3) * 4 + aw) * 272 + lane * 4) = xv[k].x;
    }
    lds_signal(ctl + C_QB);
    TS(16);
    {
      lds_wait(ctl + C_WK, 1);
      v4u wk[2][4];
#pragma unroll
      for (int t = 0; t < 2; ++t)
#pragma unroll
        for (int s = 0; s < 4; ++s) wk[t][s] = *(const LAS v4u*)(L + L_WK + ((aw * 2 + t) * 4 + s) * 1024 + lane * 16);
      lds_wait(ctl + C_QB, 4);
      f32x4 a2[2] = {f32x4{0.f, 0.f, 0.f, 0.f}, f32x4{0.f, 0.f, 0.f, 0.f}};
#pragma unroll
      for (int s = 0; s < 4; ++s) {
        const v4u qf = *(const LAS v4u*)(L + L_QP + ((r & 3) * 4 + (r >> 2)) * 272 + s * 64 + g * 16);
        a2[0] = mfma16(qf, wk[0][s], a2[0]);
        a2[1] = mfma16(qf, wk[1][s], a2[1]);
      }
#pragma unroll
      for (int t = 0; t < 2; ++t) {
        const float v = ((a2[t][0] + a2[t][1]) + (a2[t][2] + a2[t][3])) * QSCALE;
        *(LAS unsigned short*)(L + L_QA + g * 256 + (32 * aw + 16 * t + r) * 2) = (unsigned short)(pk(v, v) & 0xFFFFu);
      }
    }
    lds_signal(ctl + C_QA);
    if (aw == 0) {
      lds_wait(ctl + C_QA, 5);
      const v4u v = *(const LAS v4u*)(L + L_QA + lane * 16);
      BST(v, ws, (cq == 3 && r >= 8) ? WS_Q7 + (((p1 * 4 + g) * 4 + ((r - 8) >> 1)) * 64 + (r & 1) * 32 + h) * 16
                                     : WS_QG + ((p1 * 4 + g) * 32 + h) * 1024 + cq * 256 + r * 16);
      lds_signal(ctl + C_QST);
      TS(14);
      wait_vm<0>();
      f1tag += 1u;
      if (lane < 4) BST32(f1tag, ws, f1own);
      TS(3);
    }
    const int r32 = lane & 31, hi = lane >> 5;
    v4u ta[32];
    const unsigned tbase = L_T + (32 * aw + r32) * 256;
    const unsigned tswz = ((r32 & 3) << 2) | ((r32 >> 2) & 3);
#define TA_LOAD(q) do { _Pragma("unroll") for (int s8 = 0; s8 < 8; ++s8) \
      ta[8 * (q) + s8] = *(const LAS v4u*)(L + tbase + (q) * 32768 + (((2 * s8 + hi) ^ tswz) << 4)); SB(); } while (0)
    lds_wait_slow(ctl + C_KV0, 1);
    TA_LOAD(0);
    lds_wait_slow(ctl + C_KV1, 1);
    TA_LOAD(1);
    if (aw == 0) {
      bool c0 = false, c1 = false;
      lds_wait_slow(ctl + C_PN, 1);
      for (unsigned spins = 0; spins < SPIN_CAP; ++spins) {
        CFENCE();
        const v2u f = BLD64(ws, WS_F1 + pb * 512 + lane * 8, 0);
        c0 = c0 || f.x != f1s.x; c1 = c1 || f.y != f1s.y;
        if (__all(c0 && c1)) break;
        if ((spins & 1u) == 1u) HEARTBEAT(true);
        __builtin_amdgcn_s_sleep(FS);
      }
      lds_signal(ctl + C_F1);
    } else {
      CFENCE();
      for (unsigned spins = 0; spins < LDS_CAP && lds_peek(ctl + C_F1) < 1u; ++spins) {
        __builtin_amdgcn_s_sleep(1);
        if ((spins & 127u) == 127u) HEARTBEAT(false);
      }
      CFENCE();
    }
    TS(4);
#define QRD(s) (*(const LAS v4u*)(L + L_SG + ((s) >> 2) * 4096 + r32 * 128 + (((2 * ((s) & 3) + hi) ^ ((r32 >> 1) & 7)) << 4)))
    v4u q7[4];
    {
      const int qrow = 8 * aw + (lane >> 3);
      v4u sg[7];
#pragma unroll
      for (int s = 0; s < 7; ++s) sg[s] = BLD(ws, qrow * 1024 + (lane & 7) * 16 + s * 128, WS_QG + pb * 32768, SC1);
#pragma unroll
      for (int s = 0; s < 4; ++s) q7[s] = BLD(ws, lane * 16 + s * 1024, WS_Q7 + pb * 4096, SC1);
      SB();
      lds_wait_slow(ctl + C_KV2, 1);
      TA_LOAD(2);
#pragma unroll
      for (int s = 0; s < 7; ++s) *(LAS v4u*)(L + L_SG + s * 4096 + qrow * 128 + (((lane & 7) ^ ((qrow >> 1) & 7)) << 4)) = sg[s];
      lds_signal(ctl + C_QS);
    }
    TS(5);
    lds_wait(ctl + C_KV3, 1);
    TA_LOAD(3);
    TS(6);
    lds_wait(ctl + C_QS, 4);
    TS(17);
    s16x4 tf[8][4][2];
    const int q4 = (lane & 15) >> 2, pp = lane & 3, blk = (lane >> 4) & 1;
#define TR_LOAD(ks, ct) do { _Pragma("unroll") for (int t = 0; t < 2; ++t) \
      tf[ks][ct][t] = __builtin_amdgcn_ds_read_tr16_b64_v4i16((LAS s16x4*)(L + L_T + aw * 32768 + (ks) * 4096 + (8 * t + 4 * hi + q4) * 256 + \
          (((ct) ^ q4) << 6) + (((2 * blk + (pp >> 1)) ^ ((2 * t + hi) & 3)) << 4) + ((pp & 1) << 3))); } while (0)
    f32x16 sa;
#pragma unroll
    for (int i = 0; i < 16; ++i) sa[i] = 0.f;
    {
      v4u qf[4];
#pragma unroll
      for (int s = 0; s < 4; ++s) qf[s] = QRD(s);
      SB();
#pragma unroll
      for (int s = 0; s < 32; ++s) {
        sa = mfma32(ta[s], s < 28 ? qf[s & 3] : q7[s & 3], sa);
        if (s + 4 < 28) qf[s & 3] = QRD(s + 4);
        if (s >= 1) TR_LOAD(((s - 1) >> 1) & 7, ((s - 1) >> 4) * 2 + ((s - 1) & 1));
        SB();
      }
      TR_LOAD(7, 3);
    }
    TC(22);
    float mx = sa[0];
#pragma unroll
    for (int i = 1; i < 16; ++i) mx = fmaxf(mx, sa[i]);
    mx = xor32_max(mx);
    *(LAS float*)(L + L_MX + r32 * 16 + aw * 4) = mx;
    lds_signal(ctl + C_MX);
    TC(23);
    lds_wait(ctl + C_MX, 4);
    TC(24);
    {
      const f32x4 m4 = *(const LAS f32x4*)(L + L_MX + r32 * 16);
      mx = fmaxf(fmaxf(m4[0], m4[1]), fmaxf(m4[2], m4[3]));
    }
    {
#pragma unroll
      for (int i = 0; i < 16; ++i) sa[i] = __builtin_amdgcn_exp2f(sa[i] - mx);
      *(LAS v4u*)(L + L_P + (2 * aw) * 1024 + lane * 16) = v4u{pk(sa[0], sa[1]), pk(sa[2], sa[3]), pk(sa[4], sa[5]), pk(sa[6], sa[7])};
      *(LAS v4u*)(L + L_P + (2 * aw + 1) * 1024 + lane * 16) = v4u{pk(sa[8], sa[9]), pk(sa[10], sa[11]), pk(sa[12], sa[13]), pk(sa[14], sa[15])};
      lds_signal(ctl + C_P);
      SB();
      float ps = (((sa[0] + sa[1]) + (sa[2] + sa[3])) + ((sa[4] + sa[5]) + (sa[6] + sa[7]))) +
                 (((sa[8] + sa[9]) + (sa[10] + sa[11])) + ((sa[12] + sa[13]) + (sa[14] + sa[15])));
      ps = xor32_sum(ps);
      *(LAS float*)(L + L_LS + r32 * 16 + aw * 4) = ps;
      lds_signal(ctl + C_LS);
    }
    TC(25);
    lds_wait(ctl + C_P, 4);
    TC(26);
    TS(7);
    v4u pf[8];
#pragma unroll
    for (int ks = 0; ks < 8; ++ks) pf[ks] = *(const LAS v4u*)(L + L_P + ks * 1024 + lane * 16);
    f32x16 oa[4];
#pragma unroll
    for (int ct = 0; ct < 4; ++ct)
#pragma unroll
      for (int i = 0; i < 16; ++i) oa[ct][i] = 0.f;
#define PV_MFMA(ks, ct) oa[ct] = mfma32(__builtin_bit_cast(v4u, __builtin_shufflevector(tf[ks][ct][0], tf[ks][ct][1], 0, 1, 2, 3, 4, 5, 6, 7)), pf[ks], oa[ct])
#define O_WRITE(half, ct, gg) *(LAS v2u*)(L + L_T + aw * 32768 + (half) * 4096 + r32 * 128 + (((4 * ((ct) & 1) + (gg)) ^ (r32 & 7)) << 4) + hi * 8) = \
      v2u{pk(oa[ct][4 * (gg)], oa[ct][4 * (gg) + 1]), pk(oa[ct][4 * (gg) + 2], oa[ct][4 * (gg) + 3])}
    const unsigned ord = L_T + aw * 32768 + (lane >> 3) * 128 + (((lane & 7) ^ ((lane >> 3) & 7)) << 4);
    const unsigned ost = WS_OG + (oblk + ((lane >> 3) * 4 + aw) * 128) * 256 + (lane & 7) * 16;
#pragma unroll
    for (int ks = 0; ks < 8; ++ks) { PV_MFMA(ks, 0); PV_MFMA(ks, 1); }
    SB();
    {
      v4u ov[4];
#pragma unroll
      for (int ks = 0; ks < 8; ++ks) {
        PV_MFMA(ks, 2);
        if (ks >= 1 && ks <= 4) { O_WRITE(0, 0, ks - 1); }
        SB();
        PV_MFMA(ks, 3);
        if (ks >= 1 && ks <= 4) { O_WRITE(0, 1, ks - 1); }
        if (ks == 5) {
          CFENCE();
#pragma unroll
          for (int u = 0; u < 4; ++u) ov[u] = *(const LAS v4u*)(L + ord + u * 1024);
        }
        if (ks == 7) {
#pragma unroll
          for (int u = 0; u < 4; ++u) __builtin_amdgcn_raw_buffer_store_b128(ov[u], ws, (int)ost, u * 32 * 32768, SC1);
        }
        SB();
      }
    }
    TC(27);
    TS(8);
#pragma unroll
    for (int gg = 0; gg < 4; ++gg) { O_WRITE(1, 2, gg); O_WRITE(1, 3, gg); }
    CFENCE();
    {
      v4u ov[4];
#pragma unroll
      for (int u = 0; u < 4; ++u) ov[u] = *(const LAS v4u*)(L + ord + 4096 + u * 1024);
#pragma unroll
      for (int u = 0; u < 4; ++u) __builtin_amdgcn_raw_buffer_store_b128(ov[u], ws, (int)(ost + 128), u * 32 * 32768, SC1);
    }
    TS(15);
    TC(29);
    {
      lds_wait(ctl + C_LS, 4);
      const f32x4 l4 = *(const LAS f32x4*)(L + L_LS + r32 * 16);
      const float lsum = (l4[0] + l4[1]) + (l4[2] + l4[3]);
      wait_vm<0>();
      if (hi == 0) BST64((v2u{__float_as_uint(mx), __float_as_uint(lsum) | 1u}), ws, WS_F2 + (oblk + (r32 * 4 + aw) * 128) * 8);
    }
    TC(30);
    TS(9);
    {
      v4u st[4];
      __builtin_amdgcn_s_sleep(DS0);
      const unsigned fo = WS_F2 + blk1 * 1024 + aw * 256 + g * 64;
      for (unsigned spins = 0; spins < SPIN_CAP; ++spins) {
        CFENCE();
#pragma unroll
        for (int k = 0; k < 4; ++k) st[k] = BLD(ws, fo + k * 16, 0, SC1);
        bool ok = true;
#pragma unroll
        for (int k = 0; k < 4; ++k) ok = ok && (st[k].y != 0u) && (st[k].w != 0u);
        if (__all(ok)) break;
        if ((spins & 7u) == 7u) HEARTBEAT(true);
        __builtin_amdgcn_s_sleep(DS);
      }
      CFENCE();
      TS(10);
      v4u od[8];
#pragma unroll
      for (int i = 0; i < 8; ++i) od[i] = BLD(ws, (8 * g + i) * 256 + r * 16, WS_OG + blk1 * 32768 + aw * 8192, SC1);
      SB();
      lds_signal(ctl + C_OL);
      float m[8], l[8];
#pragma unroll
      for (int k = 0; k < 4; ++k) {
        m[2 * k] = __uint_as_float(st[k].x); l[2 * k] = __uint_as_float(st[k].y);
        m[2 * k + 1] = __uint_as_float(st[k].z); l[2 * k + 1] = __uint_as_float(st[k].w);
      }
      float M = m[0];
#pragma unroll
      for (int i = 1; i < 8; ++i) M = fmaxf(M, m[i]);
      M = xor32_max(xor16_max(M));
      float Ls = 0.f;
#pragma unroll
      for (int i = 0; i < 8; ++i) { m[i] = __builtin_amdgcn_exp2f(m[i] - M); Ls += m[i] * l[i]; }
      Ls = xor32_sum(xor16_sum(Ls));
      const float inv = 1.0f / Ls;
      v4u wf;
#pragma unroll
      for (int k = 0; k < 4; ++k) {
        const float w0 = m[2 * k] * inv, w1 = m[2 * k + 1] * inv;
        const unsigned hi2 = pk(w0, w1);
        const unsigned lo2 = pk(w0 - __uint_as_float(hi2 << 16), w1 - __uint_as_float(hi2 & 0xFFFF0000u));
        wf[k] = (r & 1) ? lo2 : hi2;
      }
      SB();
      f32x4 la[8];
#pragma unroll
      for (int e = 0; e < 8; ++e) la[e] = mfma16(wf, tcol(od, e), f32x4{0.f, 0.f, 0.f, 0.f});
      const v4u o = {pk(la[0][0] + la[0][1], la[1][0] + la[1][1]), pk(la[2][0] + la[2][1], la[3][0] + la[3][1]),
                     pk(la[4][0] + la[4][1], la[5][0] + la[5][1]), pk(la[6][0] + la[6][1], la[7][0] + la[7][1])};
      if (g == 0) *(LAS v4u*)(L + L_QA + aw * 256 + r * 16) = o;
      lds_signal(ctl + C_LAT);
      TS(11);
    }
    if (aw == 0) {
      const int eb = h & 3, em = 4 * ((h >> 2) & 1) + (h >> 3);
      lds_wait_slow(ctl + C_WST, 4);
      v4u pv;
      const unsigned po = WS_X1 + (p1 * 128 + (lane >> 1) * 4 + cq) * 2048 + 1024 + eb * 256 + em * 32 + (lane & 1) * 16;
      for (unsigned spins = 0; spins < SPIN_CAP; ++spins) {
        CFENCE();
        pv = BLD(ws, po, 0, SC1);
        if (__all(pv.x != SENT && pv.y != SENT && pv.z != SENT && pv.w != SENT)) break;
        __builtin_amdgcn_s_sleep(1);
      }
      TS(12);
      float s[8];
#pragma unroll
      for (int c = 0; c < 4; ++c) { s[2 * c] = __uint_as_float(pv[c] << 16); s[2 * c + 1] = __uint_as_float(pv[c] & 0xFFFF0000u); }
#pragma unroll
      for (int c = 0; c < 8; ++c) {
        float v = s[c];
        v = dpp_add<0x4E>(v);
        v = dpp_add<0x124>(v);
        v = dpp_add<0x128>(v);
        v = xor32_sum(xor16_sum(v));
        s[c] = v;
      }
      if (lane < 2) {
        const v4u o = {pk(s[0], s[1]), pk(s[2], s[3]), pk(s[4], s[5]), pk(s[6], s[7])};
        *(GAS v4u*)((char*)P.out + (eb * 1024 + p1 * 512 + cq * 128 + em * 16 + lane * 8) * 2) = o;
      }
      TS(13);
    }
  } else {
    const int lw = wave - 4;
    if (lw == 0) {
      const unsigned lds0 = (unsigned)(size_t)L;
      const unsigned long long tk0 = __builtin_amdgcn_s_memtime();
      __builtin_amdgcn_s_setprio(3);
      lds_wait(ctl + C_QP, 1);
      {
        const char* wkb = (const char*)P.wuk[p1] + (size_t)(128 * cq) * 8192 + h * 256;
        const unsigned vk = r * 8192 + g * 16;
        for (int G = 0; G < 4; ++G) {
#pragma unroll
          for (int i = 0; i < 8; ++i) dma16(wkb + (size_t)(G * 2 + (i >> 2)) * 16 * 8192 + (i & 3) * 64, vk, lds0 + L_WK + (G * 8 + i) * 1024);
        }
        wait_vm<0>();
        lds_signal(ctl + C_WK);
        while (lds_peek(ctl + C_QST) < 1u && __builtin_amdgcn_s_memtime() - tk0 < (unsigned long long)TRES) __builtin_amdgcn_s_sleep(1);
        CFENCE();
      }
      const char* kvb = (const char*)P.ckv[p2] + ((size_t)b2 * 4096 + 128 * j) * 1024;
      unsigned vo[4];
#pragma unroll
      for (int u3 = 0; u3 < 4; ++u3) vo[u3] = g * 1024 + ((r ^ ((g << 2) | u3)) << 4);
      for (int G = 0; G < 16; ++G) {
        wait_vm<KDEPTH - 8>();
        const int done = 8 * G - (KDEPTH - 8);
        if (G == 12) lds_wait_slow(ctl + C_QST, 1);
        if (G == PG) lds_signal(ctl + C_PN);
        if (done == 32) lds_signal(ctl + C_KV0);
        if (done == 64) lds_signal(ctl + C_KV1);
        if (done == 96) lds_signal(ctl + C_KV2);
        const int q = G >> 2, u0 = (G & 3) * 8;
        const char* gb = kvb + u0 * 4096 + q * 256;
        const unsigned lb = lds0 + L_T + q * 32768 + u0 * 1024;
#pragma unroll
        for (int i = 0; i < 8; ++i) dma16(gb + i * 4096, vo[i & 3], lb + i * 1024);
      }
      TS(1);
      if (120 - (KDEPTH - 8) < 96) { wait_vm<32>(); lds_signal(ctl + C_KV2); }
      wait_vm<0>();
      lds_signal(ctl + C_KV3);
      __builtin_amdgcn_s_setprio(0);
      TS(2);
    } else if (lw == 3) {
      {
        const v4u z = {0u, 0u, 0u, 0u}, sn = {SENT, SENT, SENT, SENT};
        BST(z, ws, WS_F2 + blk1 * 1024 + lane * 16);
        BST(sn, ws, WS_X1 + blk1 * 2048 + lane * 16);
        BST(sn, ws, WS_X1 + blk1 * 2048 + 1024 + lane * 16);
        wait_vm<0>();
        lds_signal(ctl + C_QA);
      }
    }
    const __amdgpu_buffer_rsrc_t rv = RSRC(P.wuv[p1], 512 * 4096 * 2);
    const __amdgpu_buffer_rsrc_t ro = RSRC(P.wo[p1], 512 * 4096 * 2);
    v4u wv[8], wo[8];
    if (WISS) lds_wait_slow(ctl + C_KV3, 1);
    lds_wait_slow(ctl + C_QS, 4);
#pragma unroll
    for (int jj = 0; jj < 8; ++jj) wv[jj] = BLD(rv, g * 8 * 8192 + h * 256 + r * 16, (128 * cq + 32 * lw + jj) * 8192, NT);
    SB();
    TS(3);
    v4u tv[8], to[8];
#pragma unroll
    for (int i = 0; i < 8; ++i) { tv[i] = tcol(wv, i); asm volatile("" : "+v"(tv[i])); }
    lds_wait_slow(ctl + C_OL, 4);
#pragma unroll
    for (int jj = 0; jj < 8; ++jj) wo[jj] = BLD(ro, g * 8 * 1024 + r * 16, (h * 128 + 32 * lw + jj) * 1024 + cq * 256, NT);
    SB();
    TS(4);
    lds_wait_slow(ctl + C_LAT, 4);
    {
      const v4u lf = *(const LAS v4u*)(L + L_QA + (r >> 2) * 256 + lw * 64 + g * 16);
      f32x4 ca[8];
#pragma unroll
      for (int i = 0; i < 8; ++i) ca[i] = mfma16(lf, tv[i], f32x4{0.f, 0.f, 0.f, 0.f});
      *(LAS f32x4*)(L + L_RED + (lw * 4 + g) * 512 + r * 32) = f32x4{ca[0][0], ca[1][0], ca[2][0], ca[3][0]};
      *(LAS f32x4*)(L + L_RED + (lw * 4 + g) * 512 + r * 32 + 16) = f32x4{ca[4][0], ca[5][0], ca[6][0], ca[7][0]};
      lds_signal(ctl + C_CTX);
      lds_wait(ctl + C_CTX, 4);
      f32x2 s = *(const LAS f32x2*)(L + L_RED + lw * 512 + lane * 8);
#pragma unroll
      for (int k = 1; k < 4; ++k) s += *(const LAS f32x2*)(L + L_RED + (k * 4 + lw) * 512 + lane * 8);
      const unsigned cv = pk_ns(s[0], s[1]);
      BST32(cv, ws, WS_X1 + blk1 * 2048 + lw * 256 + lane * 4);
      *(LAS unsigned*)(L + L_QP + (cq * 4 + lw) * 272 + lane * 4) = cv;
    }
    TS(5);
#pragma unroll
    for (int i = 0; i < 8; ++i) { to[i] = tcol(wo, i); asm volatile("" : "+v"(to[i])); }
    {
      unsigned xv[3];
      const unsigned xb = WS_X1 + (p1 * 128 + h * 4) * 2048 + lw * 256 + lane * 4;
      for (unsigned spins = 0; spins < SPIN_CAP; ++spins) {
        CFENCE();
#pragma unroll
        for (int k = 0; k < 3; ++k) xv[k] = BLD32(ws, xb, ((cq + 1 + k) & 3) * 2048);
        if (__all(xv[0] != SENT && xv[1] != SENT && xv[2] != SENT)) break;
        __builtin_amdgcn_s_sleep(1);
      }
#pragma unroll
      for (int k = 0; k < 3; ++k) *(LAS unsigned*)(L + L_QP + (((cq + 1 + k) & 3) * 4 + lw) * 272 + lane * 4) = xv[k];
    }
    lds_signal(ctl + C_X1);
    TS(7);
    lds_wait(ctl + C_X1, 4);
    {
      const v4u cf = *(const LAS v4u*)(L + L_QP + ((r & 3) * 4 + (r >> 2)) * 272 + lw * 64 + g * 16);
      float o[8];
#pragma unroll
      for (int i = 0; i < 8; ++i) {
        const f32x4 a = mfma16(cf, to[i], f32x4{0.f, 0.f, 0.f, 0.f});
        o[i] = (a[0] + a[1]) + (a[2] + a[3]);
      }
      *(LAS f32x4*)(L + L_RED3 + (lw * 4 + g) * 512 + r * 32) = f32x4{o[0], o[1], o[2], o[3]};
      *(LAS f32x4*)(L + L_RED3 + (lw * 4 + g) * 512 + r * 32 + 16) = f32x4{o[4], o[5], o[6], o[7]};
      lds_signal(ctl + C_O3);
      lds_wait(ctl + C_O3, 4);
      f32x2 s = *(const LAS f32x2*)(L + L_RED3 + lw * 512 + lane * 8);
#pragma unroll
      for (int k = 1; k < 4; ++k) s += *(const LAS f32x2*)(L + L_RED3 + (k * 4 + lw) * 512 + lane * 8);
      BST32(pk_ns(s[0], s[1]), ws, WS_X1 + blk1 * 2048 + 1024 + lw * 256 + lane * 4);
    }
    lds_signal(ctl + C_WST);
    TS(6);
  }
}

extern "C" void kernel_launch(void* const* d_in, const int* in_sizes, int n_in, void* d_out, int out_size, void* d_ws,
                              size_t ws_size, hipStream_t stream) {
  Params P;
  for (int p = 0; p < 2; ++p) {
    P.x[p] = d_in[6 * p + 0]; P.ckv[p] = d_in[6 * p + 1]; P.wq[p] = d_in[6 * p + 2];
    P.wuk[p] = d_in[6 * p + 3]; P.wuv[p] = d_in[6 * p + 4]; P.wo[p] = d_in[6 * p + 5];
  }
  P.out = d_out; P.ws = d_ws;
  (void)hipFuncSetAttribute((const void*)mla_fused, hipFuncAttributeMaxDynamicSharedMemorySize, (int)L_END);
  mla_fused<<<256, 512, L_END, stream>>>(P);
}
```

```cpp
#include <hip/hip_runtime.h>
#include <hip/hip_bf16.h>

#define GAS __attribute__((address_space(1)))
#define LAS __attribute__((address_space(3)))
typedef unsigned v4u __attribute__((ext_vector_type(4)));
typedef unsigned v2u __attribute__((ext_vector_type(2)));
typedef float f32x2 __attribute__((ext_vector_type(2)));
typedef float f32x4 __attribute__((ext_vector_type(4)));
typedef float f32x16 __attribute__((ext_vector_type(16)));
typedef short bf16x8 __attribute__((ext_vector_type(8)));
typedef short s16x4 __attribute__((ext_vector_type(4)));
#define RLX_WG __ATOMIC_RELAXED, __HIP_MEMORY_SCOPE_WORKGROUP
#define DI __device__ __forceinline__

#define KDEPTH 40
#define KB1 0
#define WSTART 6
#define WDEPTH 8
#define PSLEEP 2
#define APRIO 1
#define XS0 24
#define XS 8
#define PG 12
#define FS 16
#define DS0 16
#define DS 6
#define KWAITQ 1
#define TRES 7000
#define QEARLY 0
#define STAMPS 0
#define DELAY_TEST 0

constexpr unsigned WS_F1 = 0;
constexpr unsigned WS_XG = 4096;
constexpr unsigned WS_QG = WS_XG + 64 * 4 * 2048;
constexpr unsigned WS_Q7 = WS_QG + 8 * 32768;
constexpr unsigned WS_F2 = WS_Q7 + 8 * 4096;
constexpr unsigned WS_X1 = WS_F2 + 256 * 1024;
constexpr unsigned WS_OG = WS_X1 + 256 * 2048;
constexpr unsigned WS_ST = WS_OG + 256 * 32768;
constexpr unsigned WS_END = WS_ST + 256 * 8 * 32 * 8;

constexpr unsigned L_T = 0;
constexpr unsigned L_WK = 3 * 32768;
constexpr unsigned L_CT = 131072;
constexpr unsigned L_MX = L_CT + 128;
constexpr unsigned L_QA = L_MX + 512;
constexpr unsigned L_SG = L_QA + 1024;
constexpr unsigned L_RED = L_SG;
constexpr unsigned L_QP = L_SG + 8192;
constexpr unsigned L_P = L_SG + 16384;
constexpr unsigned L_LS = L_SG + 24576;
constexpr unsigned L_RED3 = L_SG + 16384;
constexpr unsigned L_END = L_SG + 28 * 1024;
enum { C_QP = 0, C_QB, C_OD, C_QA, C_F1, C_QS, C_KV0, C_KV1, C_KV2, C_KV3, C_ISS, C_MX, C_P, C_LAT, C_CTX, C_WST, C_ABI,
       C_WK, C_QST, C_X1, C_O3, C_PN, C_QR, C_QS2, C_LS, C_OL, C_N };

constexpr unsigned SENT = 0xFFFFFFFFu;
constexpr unsigned SPIN_CAP = 1u << 16;
constexpr unsigned LDS_CAP = 1u << 22;
constexpr float QSCALE = 0.08838834764831845f * 1.4426950408889634f;

struct Params {
  const void* x[2]; const void* ckv[2]; const void* wq[2]; const void* wuk[2]; const void* wuv[2]; const void* wo[2];
  void* out; void* ws;
};

DI f32x4 mfma16(v4u a, v4u b, f32x4 c) {
  return __builtin_amdgcn_mfma_f32_16x16x32_bf16(__builtin_bit_cast(bf16x8, a), __builtin_bit_cast(bf16x8, b), c, 0, 0, 0);
}
DI f32x16 mfma32(v4u a, v4u b, f32x16 c) {
  return __builtin_amdgcn_mfma_f32_32x32x16_bf16(__builtin_bit_cast(bf16x8, a), __builtin_bit_cast(bf16x8, b), c, 0, 0, 0);
}
typedef __bf16 bf2_t __attribute__((ext_vector_type(2)));
DI unsigned pk(float lo, float hi) {
  f32x2 f = {lo, hi};
  return __builtin_bit_cast(unsigned, __builtin_convertvector(f, bf2_t));
}
DI unsigned pk_ns(float lo, float hi) {
  const unsigned u = pk(lo, hi);
  return (u == SENT) ? 0x7FC07FC0u : u;
}
DI v4u tcol(const v4u (&w)[8], int i) {
  const unsigned sel = (i & 1) ? 0x07060302u : 0x05040100u;
  v4u o;
  o.x = __builtin_amdgcn_perm(w[1][i >> 1], w[0][i >> 1], sel);
  o.y = __builtin_amdgcn_perm(w[3][i >> 1], w[2][i >> 1], sel);
  o.z = __builtin_amdgcn_perm(w[5][i >> 1], w[4][i >> 1], sel);
  o.w = __builtin_amdgcn_perm(w[7][i >> 1], w[6][i >> 1], sel);
  return o;
}
#define CFENCE() asm volatile("" ::: "memory")
#define SB() __builtin_amdgcn_sched_barrier(0)
DI void lds_signal(LAS unsigned* c, int lane) {
  unsigned long long keep;
  asm volatile("s_mov_b64 %0, exec\n\ts_mov_b64 exec, 1\n\tds_add_u32 %1, %2\n\ts_mov_b64 exec, %0"
               : "=&s"(keep) : "v"((unsigned)(size_t)c), "v"(1u) : "memory");
}
DI unsigned lds_peek(LAS unsigned* c) { return (unsigned)__builtin_amdgcn_readfirstlane(__hip_atomic_load(c, RLX_WG)); }
DI void lds_wait_slow(LAS unsigned* c, unsigned target) {
  CFENCE();
  for (unsigned n = 0; n < LDS_CAP && lds_peek(c) < target; ++n) __builtin_amdgcn_s_sleep(1);
  CFENCE();
}
DI void lds_wait(LAS unsigned* c, unsigned target) {
  CFENCE();
  for (unsigned n = 0; n < LDS_CAP && lds_peek(c) < target; ++n) {}
  CFENCE();
}
DI float xor32_max(float v) {
  auto r = __builtin_amdgcn_permlane32_swap(__float_as_uint(v), __float_as_uint(v), false, false);
  return fmaxf(__uint_as_float(r[0]), __uint_as_float(r[1]));
}
DI float xor32_sum(float v) {
  auto r = __builtin_amdgcn_permlane32_swap(__float_as_uint(v), __float_as_uint(v), false, false);
  return __uint_as_float(r[0]) + __uint_as_float(r[1]);
}
DI float xor16_max(float v) {
  auto r = __builtin_amdgcn_permlane16_swap(__float_as_uint(v), __float_as_uint(v), false, false);
  return fmaxf(__uint_as_float(r[0]), __uint_as_float(r[1]));
}
DI float xor16_sum(float v) {
  auto r = __builtin_amdgcn_permlane16_swap(__float_as_uint(v), __float_as_uint(v), false, false);
  return __uint_as_float(r[0]) + __uint_as_float(r[1]);
}
template <int CTRL> DI float dpp_add(float v) {
  return v + __uint_as_float(__builtin_amdgcn_update_dpp(0u, __float_as_uint(v), CTRL, 0xF, 0xF, false));
}
DI void dma16(const void* sbase, unsigned voff, unsigned lds_dst) {
  unsigned keep;
  asm volatile("s_mov_b32 %0, m0\n\ts_mov_b32 m0, %3\n\ts_nop 0\n\tglobal_load_lds_dwordx4 %1, %2 nt\n\ts_mov_b32 m0, %0"
               : "=&s"(keep) : "v"(voff), "s"(sbase), "s"(lds_dst) : "memory");
}
template <int N> DI void wait_vm() { asm volatile("s_waitcnt vmcnt(%0)" ::"i"(N) : "memory"); }
#define STAMP(i) do {} while (0)
#define TS(i) do {} while (0)
#define TC(i) do {} while (0)
#define RSRC(ptr, bytes) __builtin_amdgcn_make_buffer_rsrc((void*)(ptr), 0, (int)(bytes), 0x00020000)
#define BLD(rs, vo, so, aux) __builtin_amdgcn_raw_buffer_load_b128(rs, (int)(vo), (int)(so), aux)
#define BLD64(rs, vo, so) __builtin_amdgcn_raw_buffer_load_b64(rs, (int)(vo), (int)(so), SC1)
#define BLD32(rs, vo, so) __builtin_amdgcn_raw_buffer_load_b32(rs, (int)(vo), (int)(so), SC1)
#define BST(v, rs, vo) __builtin_amdgcn_raw_buffer_store_b128(v, rs, (int)(vo), 0, SC1)
#define BST64(v, rs, vo) __builtin_amdgcn_raw_buffer_store_b64(v, rs, (int)(vo), 0, SC1)
#define BST32(v, rs, vo) __builtin_amdgcn_raw_buffer_store_b32(v, rs, (int)(vo), 0, SC1)
constexpr int SC1 = 16, NT = 2;

__global__ void __launch_bounds__(512, 2) mla_fused(Params P) {
  extern __shared__ __attribute__((aligned(16))) unsigned char smem[];
  LAS unsigned char* L = (LAS unsigned char*)smem;
  LAS unsigned* ctl = (LAS unsigned*)(L + L_CT);
  const int tid = threadIdx.x, lane = tid & 63;
  const int wave = __builtin_amdgcn_readfirstlane(tid >> 6);
  const int w = blockIdx.x;
  const int ph = w & 63, p1 = ph >> 5, h = w & 31, cq = w >> 6;
  const int src = h * 4 + cq;
  const int blk1 = p1 * 128 + src;
  const int pb = w & 7, p2 = pb >> 2, b2 = pb & 3, j = w >> 3;
  const int r = lane & 15, g = lane >> 4;
  const __amdgpu_buffer_rsrc_t ws = RSRC(P.ws, WS_END);
  TS(0);
  if (tid < C_N) ctl[tid] = 0u;
  asm volatile("s_waitcnt lgkmcnt(0)" ::: "memory");
  __builtin_amdgcn_s_barrier();

  if (wave < 4) {
    const int aw = wave;
    __builtin_amdgcn_s_setprio(APRIO);
    const __amdgpu_buffer_rsrc_t rx = RSRC(P.x[p1], 4 * 512 * 2);
    const __amdgpu_buffer_rsrc_t rq = RSRC(P.wq[p1], 512 * 4096 * 2);
    const unsigned xo = WS_XG + ph * 8192 + tid * 8;
    const unsigned f1own = WS_F1 + ((p1 * 4 + (lane & 3)) * 128 + src) * 4;
    unsigned xtag = BLD32(ws, xo + cq * 2048 + 4, 0);
    unsigned xs[3];
#pragma unroll
    for (int k = 0; k < 3; ++k) xs[k] = BLD32(ws, xo + 4, ((cq + 1 + k) & 3) * 2048);
    v2u f1s = {0u, 0u};
    unsigned f1tag = 0u;
    if (aw == 0) { f1s = BLD64(ws, WS_F1 + pb * 512 + lane * 8, 0); f1tag = BLD32(ws, f1own, 0); }
    SB();
    v4u xf, wq[8];
    const unsigned vq = g * 8 * 8192 + h * 256 + r * 16;
#pragma unroll
    for (int jj = 0; jj < 8; ++jj) wq[jj] = BLD(rq, vq, (128 * cq + 32 * aw + jj) * 8192, NT);
    xf = BLD(rx, (r >> 2) * 1024 + g * 16, (128 * cq + 32 * aw) * 2, 0);
    SB();
    lds_signal(ctl + C_ABI, lane);
    TS(1);
    unsigned xval;
    {
      f32x4 acc[8];
#pragma unroll
      for (int i = 0; i < 8; ++i) acc[i] = mfma16(xf, tcol(wq, i), f32x4{0.f, 0.f, 0.f, 0.f});
      *(LAS f32x4*)(L + L_RED + (aw * 4 + g) * 512 + r * 32) = f32x4{acc[0][0], acc[1][0], acc[2][0], acc[3][0]};
      *(LAS f32x4*)(L + L_RED + (aw * 4 + g) * 512 + r * 32 + 16) = f32x4{acc[4][0], acc[5][0], acc[6][0], acc[7][0]};
      lds_signal(ctl + C_QP, lane);
      lds_wait(ctl + C_QP, 4);
      f32x2 s = *(const LAS f32x2*)(L + L_RED + aw * 512 + lane * 8);
#pragma unroll
      for (int k = 1; k < 4; ++k) s += *(const LAS f32x2*)(L + L_RED + (k * 4 + aw) * 512 + lane * 8);
      xval = pk(s[0], s[1]);
    }
    xtag += 1u;
    BST64((v2u{xval, xtag}), ws, xo + cq * 2048);
    *(LAS unsigned*)(L + L_QP + (cq * 4 + aw) * 272 + lane * 4) = xval;
    TS(2);
#define HEARTBEAT(flags_too) do { xtag += 1u; BST64((v2u{xval, xtag}), ws, xo + cq * 2048); \
      if ((flags_too) && aw == 0) { f1tag += 1u; if (lane < 4) BST32(f1tag, ws, f1own); } } while (0)
    {
      v2u xv[3];
      __builtin_amdgcn_s_sleep(XS0);
      for (unsigned spins = 0; spins < SPIN_CAP; ++spins) {
        CFENCE();
#pragma unroll
        for (int k = 0; k < 3; ++k) xv[k] = BLD64(ws, xo, ((cq + 1 + k) & 3) * 2048);
        if (__all(xv[0].y != xs[0] && xv[1].y != xs[1] && xv[2].y != xs[2])) break;
        if ((spins & 3u) == 3u) HEARTBEAT(false);
        __builtin_amdgcn_s_sleep(XS);
      }
#pragma unroll
      for (int k = 0; k < 3; ++k) *(LAS unsigned*)(L + L_QP + (((cq + 1 + k) & 3) * 4 + aw) * 272 + lane * 4) = xv[k].x;
    }
    lds_signal(ctl + C_QB, lane);
    TS(16);
    {
      lds_wait(ctl + C_WK, 1);
      v4u wk[2][4];
#pragma unroll
      for (int t = 0; t < 2; ++t)
#pragma unroll
        for (int s = 0; s < 4; ++s) wk[t][s] = *(const LAS v4u*)(L + L_WK + ((aw * 2 + t) * 4 + s) * 1024 + lane * 16);
      lds_wait(ctl + C_QB, 4);
      f32x4 a2[2] = {f32x4{0.f, 0.f, 0.f, 0.f}, f32x4{0.f, 0.f, 0.f, 0.f}};
#pragma unroll
      for (int s = 0; s < 4; ++s) {
        const v4u qf = *(const LAS v4u*)(L + L_QP + ((r & 3) * 4 + (r >> 2)) * 272 + s * 64 + g * 16);
        a2[0] = mfma16(qf, wk[0][s], a2[0]);
        a2[1] = mfma16(qf, wk[1][s], a2[1]);
      }
#pragma unroll
      for (int t = 0; t < 2; ++t) {
        const float v = ((a2[t][0] + a2[t][1]) + (a2[t][2] + a2[t][3])) * QSCALE;
        *(LAS unsigned short*)(L + L_QA + g * 256 + (32 * aw + 16 * t + r) * 2) = (unsigned short)(pk(v, v) & 0xFFFFu);
      }
    }
    lds_signal(ctl + C_QA, lane);
    if (aw == 0) {
      lds_wait(ctl + C_QA, 5);
      const v4u v = *(const LAS v4u*)(L + L_QA + lane * 16);
      BST(v, ws, (cq == 3 && r >= 8) ? WS_Q7 + (((p1 * 4 + g) * 4 + ((r - 8) >> 1)) * 64 + (r & 1) * 32 + h) * 16
                                     : WS_QG + ((p1 * 4 + g) * 32 + h) * 1024 + cq * 256 + r * 16);
      lds_signal(ctl + C_QST, lane);
      TS(14);
      wait_vm<0>();
      f1tag += 1u;
      if (lane < 4) BST32(f1tag, ws, f1own);
      TS(3);
    }
    const int r32 = lane & 31, hi = lane >> 5;
    v4u ta[32];
    const unsigned tbase = L_T + (32 * aw + r32) * 256;
    const unsigned tswz = ((r32 & 3) << 2) | ((r32 >> 2) & 3);
#define TA_LOAD(q) do { _Pragma("unroll") for (int s8 = 0; s8 < 8; ++s8) \
      ta[8 * (q) + s8] = *(const LAS v4u*)(L + tbase + (q) * 32768 + (((2 * s8 + hi) ^ tswz) << 4)); SB(); } while (0)
    if (!QEARLY) {
      lds_wait_slow(ctl + C_KV0, 1);
      TA_LOAD(0);
      lds_wait_slow(ctl + C_KV1, 1);
      TA_LOAD(1);
    }
    if (aw == 0) {
      bool c0 = false, c1 = false;
      lds_wait_slow(ctl + C_PN, 1);
      for (unsigned spins = 0; spins < SPIN_CAP; ++spins) {
        CFENCE();
        const v2u f = BLD64(ws, WS_F1 + pb * 512 + lane * 8, 0);
        c0 = c0 || f.x != f1s.x; c1 = c1 || f.y != f1s.y;
        if (__all(c0 && c1)) break;
        if ((spins & 1u) == 1u) HEARTBEAT(true);
        __builtin_amdgcn_s_sleep(FS);
      }
      lds_signal(ctl + C_F1, lane);
    } else {
      CFENCE();
      for (unsigned spins = 0; spins < LDS_CAP && lds_peek(ctl + C_F1) < 1u; ++spins) {
        __builtin_amdgcn_s_sleep(1);
        if ((spins & 127u) == 127u) HEARTBEAT(false);
      }
      CFENCE();
    }
    TS(4);
    const int qrow = 8 * aw + (lane >> 3);
    const unsigned qwr = L_SG + qrow * 128 + (((lane & 7) ^ ((qrow >> 1) & 7)) << 4);
#define QRD(s) (*(const LAS v4u*)(L + L_SG + (((s) >> 2) & 7) % 7 * 4096 + r32 * 128 + (((2 * ((s) & 3) + hi) ^ ((r32 >> 1) & 7)) << 4)))
    v4u q7[4];
    {
      v4u sg[7];
#pragma unroll
      for (int s = 0; s < 7; ++s) sg[s] = BLD(ws, qrow * 1024 + (lane & 7) * 16 + s * 128, WS_QG + pb * 32768, SC1);
#pragma unroll
      for (int s = 0; s < 4; ++s) q7[s] = BLD(ws, lane * 16 + s * 1024, WS_Q7 + pb * 4096, SC1);
      SB();
      if (!QEARLY) {
        lds_wait_slow(ctl + C_KV2, 1);
        TA_LOAD(2);
      }
#pragma unroll
      for (int s = 0; s < 7; ++s) *(LAS v4u*)(L + qwr + s * 4096) = sg[s];
      lds_signal(ctl + C_QS, lane);
    }
    if (QEARLY) {
      lds_wait_slow(ctl + C_KV0, 1);
      TA_LOAD(0);
      lds_wait_slow(ctl + C_KV1, 1);
      TA_LOAD(1);
      lds_wait_slow(ctl + C_KV2, 1);
      TA_LOAD(2);
    }
    TS(5);
    lds_wait(ctl + C_KV3, 1);
    TA_LOAD(3);
    TS(6);
    lds_wait(ctl + C_QS, 4);
    TS(17);
    s16x4 tf[8][4][2];
    const int q4 = (lane & 15) >> 2, pp = lane & 3, blk = (lane >> 4) & 1;
#define TRADDR(ks, ct, t) (L_T + aw * 32768 + (ks) * 4096 + (8 * (t) + 4 * hi + q4) * 256 + (((ct) ^ q4) << 6) + \
                           (((2 * blk + (pp >> 1)) ^ ((2 * (t) + hi) & 3)) << 4) + ((pp & 1) << 3))
    f32x16 sa;
#pragma unroll
    for (int i = 0; i < 16; ++i) sa[i] = 0.f;
    {
      constexpr int PD = 4;
      v4u qf[PD];
#pragma unroll
      for (int s = 0; s < PD; ++s) qf[s] = QRD(s);
      SB();
#pragma unroll
      for (int s = 0; s < 32; ++s) {
        sa = mfma32(ta[s], s < 28 ? qf[s % PD] : q7[s & 3], sa);
        if (s + PD < 28) qf[s % PD] = QRD(s + PD);
        if (s >= 1) {
          const int n = s - 1;
          const int ct = (n >> 4) * 2 + (n & 1), ks = (n >> 1) & 7;
          tf[ks][ct][0] = __builtin_amdgcn_ds_read_tr16_b64_v4i16((LAS s16x4*)(L + TRADDR(ks, ct, 0)));
          tf[ks][ct][1] = __builtin_amdgcn_ds_read_tr16_b64_v4i16((LAS s16x4*)(L + TRADDR(ks, ct, 1)));
        }
        SB();
      }
      tf[7][3][0] = __builtin_amdgcn_ds_read_tr16_b64_v4i16((LAS s16x4*)(L + TRADDR(7, 3, 0)));
      tf[7][3][1] = __builtin_amdgcn_ds_read_tr16_b64_v4i16((LAS s16x4*)(L + TRADDR(7, 3, 1)));
    }
    TC(22);
    float mx = sa[0];
#pragma unroll
    for (int i = 1; i < 16; ++i) mx = fmaxf(mx, sa[i]);
    mx = xor32_max(mx);
    if (hi == 0) *(LAS float*)(L + L_MX + r32 * 16 + aw * 4) = mx;
    lds_signal(ctl + C_MX, lane);
    TC(23);
    lds_wait(ctl + C_MX, 4);
    TC(24);
    {
      const f32x4 m4 = *(const LAS f32x4*)(L + L_MX + r32 * 16);
      mx = fmaxf(fmaxf(m4[0], m4[1]), fmaxf(m4[2], m4[3]));
    }
    {
#pragma unroll
      for (int i = 0; i < 16; ++i) sa[i] = __builtin_amdgcn_exp2f(sa[i] - mx);
      const v4u p0 = {pk(sa[0], sa[1]), pk(sa[2], sa[3]), pk(sa[4], sa[5]), pk(sa[6], sa[7])};
      const v4u p1v = {pk(sa[8], sa[9]), pk(sa[10], sa[11]), pk(sa[12], sa[13]), pk(sa[14], sa[15])};
      *(LAS v4u*)(L + L_P + (2 * aw) * 1024 + lane * 16) = p0;
      *(LAS v4u*)(L + L_P + (2 * aw + 1) * 1024 + lane * 16) = p1v;
      lds_signal(ctl + C_P, lane);
      SB();
      float ps = (((sa[0] + sa[1]) + (sa[2] + sa[3])) + ((sa[4] + sa[5]) + (sa[6] + sa[7]))) +
                 (((sa[8] + sa[9]) + (sa[10] + sa[11])) + ((sa[12] + sa[13]) + (sa[14] + sa[15])));
      ps = xor32_sum(ps);
      if (hi == 0) *(LAS float*)(L + L_LS + r32 * 16 + aw * 4) = ps;
      lds_signal(ctl + C_LS, lane);
    }
    TC(25);
    lds_wait(ctl + C_P, 4);
    TC(26);
    TS(7);
    v4u pf[8];
#pragma unroll
    for (int ks = 0; ks < 8; ++ks) pf[ks] = *(const LAS v4u*)(L + L_P + ks * 1024 + lane * 16);
    f32x16 oa[4];
#pragma unroll
    for (int ct = 0; ct < 4; ++ct)
#pragma unroll
      for (int i = 0; i < 16; ++i) oa[ct][i] = 0.f;
#define PV_MFMA(ks, ct) oa[ct] = mfma32(__builtin_bit_cast(v4u, __builtin_shufflevector(tf[ks][ct][0], tf[ks][ct][1], 0, 1, 2, 3, 4, 5, 6, 7)), pf[ks], oa[ct])
#define O_WRITE(half, ct, gg) *(LAS v2u*)(L + L_T + aw * 32768 + (half) * 4096 + r32 * 128 + (((4 * ((ct) & 1) + (gg)) ^ (r32 & 7)) << 4) + hi * 8) = \
      v2u{pk(oa[ct][4 * (gg)], oa[ct][4 * (gg) + 1]), pk(oa[ct][4 * (gg) + 2], oa[ct][4 * (gg) + 3])}
    const unsigned ord = L_T + aw * 32768 + (lane >> 3) * 128 + (((lane & 7) ^ ((lane >> 3) & 7)) << 4);
    const unsigned ost = WS_OG + ((p2 * 128 + (lane >> 3) * 4 + aw) * 128 + b2 * 32 + j) * 256 + (lane & 7) * 16;
#pragma unroll
    for (int ks = 0; ks < 8; ++ks) { PV_MFMA(ks, 0); PV_MFMA(ks, 1); }
    SB();
    {
      v4u ov[4];
#pragma unroll
      for (int ks = 0; ks < 8; ++ks) {
        PV_MFMA(ks, 2);
        if (ks >= 1 && ks <= 4) { O_WRITE(0, 0, ks - 1); }
        SB();
        PV_MFMA(ks, 3);
        if (ks >= 1 && ks <= 4) { O_WRITE(0, 1, ks - 1); }
        if (ks == 5) {
          CFENCE();
#pragma unroll
          for (int u = 0; u < 4; ++u) ov[u] = *(const LAS v4u*)(L + ord + u * 1024);
        }
        if (ks == 7) {
#pragma unroll
          for (int u = 0; u < 4; ++u) __builtin_amdgcn_raw_buffer_store_b128(ov[u], ws, (int)ost, u * 32 * 32768, SC1);
        }
        SB();
      }
    }
    TC(27);
    TS(8);
#pragma unroll
    for (int gg = 0; gg < 4; ++gg) { O_WRITE(1, 2, gg); O_WRITE(1, 3, gg); }
    CFENCE();
    {
      v4u ov[4];
#pragma unroll
      for (int u = 0; u < 4; ++u) ov[u] = *(const LAS v4u*)(L + ord + 4096 + u * 1024);
#pragma unroll
      for (int u = 0; u < 4; ++u) __builtin_amdgcn_raw_buffer_store_b128(ov[u], ws, (int)(ost + 128), u * 32 * 32768, SC1);
    }
    lds_wait(ctl + C_LS, 4);
    const f32x4 l4 = *(const LAS f32x4*)(L + L_LS + r32 * 16);
    const float lsum = (l4[0] + l4[1]) + (l4[2] + l4[3]);
    TS(15);
    TC(29);
    wait_vm<0>();
    if (hi == 0) {
      const v2u gr = {__float_as_uint(mx), __float_as_uint(lsum) | 1u};
      BST64(gr, ws, WS_F2 + ((p2 * 128 + r32 * 4 + aw) * 128 + b2 * 32 + j) * 8);
    }
    TC(30);
    lds_signal(ctl + C_OD, lane);
    TS(9);
    {
      v4u st[4];
      __builtin_amdgcn_s_sleep(DS0);
      const unsigned fo = WS_F2 + blk1 * 1024 + aw * 256 + g * 64;
      for (unsigned spins = 0; spins < SPIN_CAP; ++spins) {
        CFENCE();
#pragma unroll
        for (int k = 0; k < 4; ++k) st[k] = BLD(ws, fo + k * 16, 0, SC1);
        bool ok = true;
#pragma unroll
        for (int k = 0; k < 4; ++k) ok = ok && (st[k].y != 0u) && (st[k].w != 0u);
        if (__all(ok)) break;
        if ((spins & 7u) == 7u) HEARTBEAT(true);
        __builtin_amdgcn_s_sleep(DS);
      }
      CFENCE();
      TS(10);
      v4u od[8];
#pragma unroll
      for (int i = 0; i < 8; ++i) od[i] = BLD(ws, (8 * g + i) * 256 + r * 16, WS_OG + blk1 * 32768 + aw * 8192, SC1);
      SB();
      if (WSTART == 6) lds_signal(ctl + C_OL, lane);
      float m[8], l[8];
#pragma unroll
      for (int k = 0; k < 4; ++k) {
        m[2 * k] = __uint_as_float(st[k].x); l[2 * k] = __uint_as_float(st[k].y);
        m[2 * k + 1] = __uint_as_float(st[k].z); l[2 * k + 1] = __uint_as_float(st[k].w);
      }
      float M = m[0];
#pragma unroll
      for (int i = 1; i < 8; ++i) M = fmaxf(M, m[i]);
      M = xor32_max(xor16_max(M));
      float Ls = 0.f;
#pragma unroll
      for (int i = 0; i < 8; ++i) { m[i] = __builtin_amdgcn_exp2f(m[i] - M); Ls += m[i] * l[i]; }
      Ls = xor32_sum(xor16_sum(Ls));
      const float inv = 1.0f / Ls;
      v4u wf;
#pragma unroll
      for (int k = 0; k < 4; ++k) {
        const float w0 = m[2 * k] * inv, w1 = m[2 * k + 1] * inv;
        const unsigned hi2 = pk(w0, w1);
        const unsigned lo2 = pk(w0 - __uint_as_float(hi2 << 16), w1 - __uint_as_float(hi2 & 0xFFFF0000u));
        wf[k] = (r & 1) ? lo2 : hi2;
      }
      SB();
      f32x4 la[8];
#pragma unroll
      for (int e = 0; e < 8; ++e) la[e] = mfma16(wf, tcol(od, e), f32x4{0.f, 0.f, 0.f, 0.f});
      const v4u o = {pk(la[0][0] + la[0][1], la[1][0] + la[1][1]), pk(la[2][0] + la[2][1], la[3][0] + la[3][1]),
                     pk(la[4][0] + la[4][1], la[5][0] + la[5][1]), pk(la[6][0] + la[6][1], la[7][0] + la[7][1])};
      if (g == 0) *(LAS v4u*)(L + L_QA + aw * 256 + r * 16) = o;
      lds_signal(ctl + C_LAT, lane);
      TS(11);
    }
    if (aw == 0) {
      const int eb = h & 3, em = 4 * ((h >> 2) & 1) + (h >> 3);
      lds_wait_slow(ctl + C_WST, 4);
      v4u pv;
      const unsigned po = WS_X1 + (p1 * 128 + (lane >> 1) * 4 + cq) * 2048 + 1024 + eb * 256 + em * 32 + (lane & 1) * 16;
      for (unsigned spins = 0; spins < SPIN_CAP; ++spins) {
        CFENCE();
        pv = BLD(ws, po, 0, SC1);
        if (__all(pv.x != SENT && pv.y != SENT && pv.z != SENT && pv.w != SENT)) break;
        __builtin_amdgcn_s_sleep(1);
      }
      TS(12);
      float s[8];
#pragma unroll
      for (int c = 0; c < 4; ++c) { s[2 * c] = __uint_as_float(pv[c] << 16); s[2 * c + 1] = __uint_as_float(pv[c] & 0xFFFF0000u); }
#pragma unroll
      for (int c = 0; c < 8; ++c) {
        float v = s[c];
        v = dpp_add<0x4E>(v);
        v = dpp_add<0x124>(v);
        v = dpp_add<0x128>(v);
        v = xor32_sum(xor16_sum(v));
        s[c] = v;
      }
      if (lane < 2) {
        const v4u o = {pk(s[0], s[1]), pk(s[2], s[3]), pk(s[4], s[5]), pk(s[6], s[7])};
        *(GAS v4u*)((char*)P.out + (eb * 1024 + p1 * 512 + cq * 128 + em * 16 + lane * 8) * 2) = o;
      }
      TS(13);
    }
  } else {
    const int lw = wave - 4;
    if (lw == 0) {
      const unsigned lds0 = (unsigned)(size_t)L;
      const unsigned long long tk0 = __builtin_amdgcn_s_memtime();
      __builtin_amdgcn_s_setprio(3);
      if (KWAITQ) lds_wait(ctl + C_QP, 1); else lds_wait(ctl + C_ABI, 4);
      {
        const char* wkb = (const char*)P.wuk[p1] + (size_t)(128 * cq) * 8192 + h * 256;
        const unsigned vk = r * 8192 + g * 16;
        for (int G = 0; G < 4; ++G) {
          wait_vm<KDEPTH - 8>();
#pragma unroll
          for (int i = 0; i < 8; ++i) dma16(wkb + (size_t)(G * 2 + (i >> 2)) * 16 * 8192 + (i & 3) * 64, vk, lds0 + L_WK + (G * 8 + i) * 1024);
        }
      }
      const char* kvb = (const char*)P.ckv[p2] + ((size_t)b2 * 4096 + 128 * j) * 1024;
      unsigned vo[4];
#pragma unroll
      for (int u3 = 0; u3 < 4; ++u3) vo[u3] = g * 1024 + ((r ^ ((g << 2) | u3)) << 4);
      for (int G = 0; G < 16; ++G) {
        if (8 * G == KB1) {
          if (KB1 <= KDEPTH - 8) { wait_vm<(KB1 < 63 ? KB1 : 63)>(); lds_signal(ctl + C_WK, lane); }
          while (lds_peek(ctl + C_QST) < 1u && __builtin_amdgcn_s_memtime() - tk0 < (unsigned long long)TRES) __builtin_amdgcn_s_sleep(1);
          CFENCE();
        }
        wait_vm<KDEPTH - 8>();
        const int done = 8 * G - (KDEPTH - 8);
        if (done == 0 && !(KB1 <= KDEPTH - 8)) lds_signal(ctl + C_WK, lane);
        if (G == 12) lds_wait_slow(ctl + C_QST, 1);
        if (G == PG) lds_signal(ctl + C_PN, lane);
        if (done == 32) lds_signal(ctl + C_KV0, lane);
        if (done == 64) lds_signal(ctl + C_KV1, lane);
        if (done == 96) lds_signal(ctl + C_KV2, lane);
        const int q = G >> 2, u0 = (G & 3) * 8;
        const char* gb = kvb + u0 * 4096 + q * 256;
        const unsigned lb = lds0 + L_T + q * 32768 + u0 * 1024;
#pragma unroll
        for (int i = 0; i < 8; ++i) dma16(gb + i * 4096, vo[i & 3], lb + i * 1024);
      }
      lds_signal(ctl + C_ISS, lane);
      if (PG >= 16) lds_signal(ctl + C_PN, lane);
      TS(1);
      if (120 - (KDEPTH - 8) < 96) { wait_vm<32>(); lds_signal(ctl + C_KV2, lane); }
      wait_vm<0>();
      lds_signal(ctl + C_KV3, lane);
      __builtin_amdgcn_s_setprio(0);
      TS(2);
    } else {
      if (lw == 3) {
        const v4u z = {0u, 0u, 0u, 0u}, sn = {SENT, SENT, SENT, SENT};
        BST(z, ws, WS_F2 + blk1 * 1024 + lane * 16);
        BST(sn, ws, WS_X1 + blk1 * 2048 + lane * 16);
        BST(sn, ws, WS_X1 + blk1 * 2048 + 1024 + lane * 16);
        wait_vm<0>();
        lds_signal(ctl + C_QA, lane);
      }
      if (WSTART == 0) lds_wait_slow(ctl + C_ISS, 1);
      if (WSTART == 1) lds_wait_slow(ctl + C_KV3, 1);
      if (WSTART >= 3) lds_wait_slow(ctl + C_ISS, 1);
    }
    if (WSTART == 2) lds_wait_slow(ctl + C_OD, 4);
    if (WSTART == 3 || WSTART == 4 || WSTART == 6) lds_wait_slow(ctl + C_QS, 4);
    const __amdgpu_buffer_rsrc_t rv = RSRC(P.wuv[p1], 512 * 4096 * 2);
    const __amdgpu_buffer_rsrc_t ro = RSRC(P.wo[p1], 512 * 4096 * 2);
    v4u wv[8], wo[8];
#pragma unroll
    for (int jj = 0; jj < 8; ++jj) wv[jj] = BLD(rv, g * 8 * 8192 + h * 256 + r * 16, (128 * cq + 32 * lw + jj) * 8192, NT);
    SB(); if (WDEPTH < 8) wait_vm<WDEPTH < 63 ? WDEPTH : 63>();
    if (WSTART == 4) lds_wait_slow(ctl + C_OD, 4);
    if (WSTART == 5) lds_wait_slow(ctl + C_QS, 4);
    v4u tv[8], to[8];
    if (WSTART == 6) {
#pragma unroll
      for (int i = 0; i < 8; ++i) { tv[i] = tcol(wv, i); asm volatile("" : "+v"(tv[i])); }
      lds_wait_slow(ctl + C_OL, 4);
    }
#pragma unroll
    for (int jj = 0; jj < 8; ++jj) wo[jj] = BLD(ro, g * 8 * 1024 + r * 16, (h * 128 + 32 * lw + jj) * 1024 + cq * 256, NT);
    SB();
    TS(3);
    if (WSTART != 6) {
#pragma unroll
      for (int i = 0; i < 8; ++i) { tv[i] = tcol(wv, i); asm volatile("" : "+v"(tv[i])); }
#pragma unroll
      for (int i = 0; i < 8; ++i) { to[i] = tcol(wo, i); asm volatile("" : "+v"(to[i])); }
    }
    TS(4);
    lds_wait_slow(ctl + C_LAT, 4);
    {
      const v4u lf = *(const LAS v4u*)(L + L_QA + (r >> 2) * 256 + lw * 64 + g * 16);
      f32x4 ca[8];
#pragma unroll
      for (int i = 0; i < 8; ++i) ca[i] = mfma16(lf, tv[i], f32x4{0.f, 0.f, 0.f, 0.f});
      *(LAS f32x4*)(L + L_RED + (lw * 4 + g) * 512 + r * 32) = f32x4{ca[0][0], ca[1][0], ca[2][0], ca[3][0]};
      *(LAS f32x4*)(L + L_RED + (lw * 4 + g) * 512 + r * 32 + 16) = f32x4{ca[4][0], ca[5][0], ca[6][0], ca[7][0]};
      lds_signal(ctl + C_CTX, lane);
      lds_wait(ctl + C_CTX, 4);
      f32x2 s = *(const LAS f32x2*)(L + L_RED + lw * 512 + lane * 8);
#pragma unroll
      for (int k = 1; k < 4; ++k) s += *(const LAS f32x2*)(L + L_RED + (k * 4 + lw) * 512 + lane * 8);
      const unsigned cv = pk_ns(s[0], s[1]);
      BST32(cv, ws, WS_X1 + blk1 * 2048 + lw * 256 + lane * 4);
      *(LAS unsigned*)(L + L_QP + (cq * 4 + lw) * 272 + lane * 4) = cv;
    }
    TS(5);
    if (WSTART == 6) {
#pragma unroll
      for (int i = 0; i < 8; ++i) { to[i] = tcol(wo, i); asm volatile("" : "+v"(to[i])); }
    }
    TS(8);
    {
      unsigned xv[3];
      const unsigned xb = WS_X1 + (p1 * 128 + h * 4) * 2048 + lw * 256 + lane * 4;
      for (unsigned spins = 0; spins < SPIN_CAP; ++spins) {
        CFENCE();
#pragma unroll
        for (int k = 0; k < 3; ++k) xv[k] = BLD32(ws, xb, ((cq + 1 + k) & 3) * 2048);
        if (__all(xv[0] != SENT && xv[1] != SENT && xv[2] != SENT)) break;
        __builtin_amdgcn_s_sleep(1);
      }
#pragma unroll
      for (int k = 0; k < 3; ++k) *(LAS unsigned*)(L + L_QP + (((cq + 1 + k) & 3) * 4 + lw) * 272 + lane * 4) = xv[k];
    }
    lds_signal(ctl + C_X1, lane);
    TS(7);
    lds_wait(ctl + C_X1, 4);
    {
      const v4u cf = *(const LAS v4u*)(L + L_QP + ((r & 3) * 4 + (r >> 2)) * 272 + lw * 64 + g * 16);
      float o[8];
#pragma unroll
      for (int i = 0; i < 8; ++i) {
        const f32x4 a = mfma16(cf, to[i], f32x4{0.f, 0.f, 0.f, 0.f});
        o[i] = (a[0] + a[1]) + (a[2] + a[3]);
      }
      *(LAS f32x4*)(L + L_RED3 + (lw * 4 + g) * 512 + r * 32) = f32x4{o[0], o[1], o[2], o[3]};
      *(LAS f32x4*)(L + L_RED3 + (lw * 4 + g) * 512 + r * 32 + 16) = f32x4{o[4], o[5], o[6], o[7]};
      lds_signal(ctl + C_O3, lane);
      lds_wait(ctl + C_O3, 4);
      f32x2 s = *(const LAS f32x2*)(L + L_RED3 + lw * 512 + lane * 8);
#pragma unroll
      for (int k = 1; k < 4; ++k) s += *(const LAS f32x2*)(L + L_RED3 + (k * 4 + lw) * 512 + lane * 8);
      BST32(pk_ns(s[0], s[1]), ws, WS_X1 + blk1 * 2048 + 1024 + lw * 256 + lane * 4);
    }
    lds_signal(ctl + C_WST, lane);
    TS(6);
  }
}

extern "C" void kernel_launch(void* const* d_in, const int* in_sizes, int n_in, void* d_out, int out_size, void* d_ws,
                              size_t ws_size, hipStream_t stream) {
  Params P;
  for (int p = 0; p < 2; ++p) {
    P.x[p] = d_in[6 * p + 0]; P.ckv[p] = d_in[6 * p + 1]; P.wq[p] = d_in[6 * p + 2];
    P.wuk[p] = d_in[6 * p + 3]; P.wuv[p] = d_in[6 * p + 4]; P.wo[p] = d_in[6 * p + 5];
  }
  P.out = d_out; P.ws = d_ws;
  (void)hipFuncSetAttribute((const void*)mla_fused, hipFuncAttributeMaxDynamicSharedMemorySize, (int)L_END);
  mla_fused<<<256, 512, L_END, stream>>>(P);
}
```

```cpp
#include <hip/hip_runtime.h>
#include <hip/hip_bf16.h>

#define GAS __attribute__((address_space(1)))
#define LAS __attribute__((address_space(3)))
typedef unsigned v4u __attribute__((ext_vector_type(4)));
typedef unsigned v2u __attribute__((ext_vector_type(2)));
typedef float f32x2 __attribute__((ext_vector_type(2)));
typedef float f32x4 __attribute__((ext_vector_type(4)));
typedef float f32x16 __attribute__((ext_vector_type(16)));
typedef short bf16x8 __attribute__((ext_vector_type(8)));
typedef short s16x4 __attribute__((ext_vector_type(4)));
#define RLX_WG __ATOMIC_RELAXED, __HIP_MEMORY_SCOPE_WORKGROUP
#define DI __device__ __forceinline__

#define KDEPTH 40
#define PG 12
#define XS0 24
#define XS 8
#define FS 16
#define DS0 16
#define DS 6
#define XPRIO 1
#define TRES 7000
#define WISS 1
#define CODEPF 2
#define CODE_BACK 0
#define CODE_KB 16
#define STAMPS 0
#define DELAY_TEST 0

constexpr unsigned WS_F1 = 0;
constexpr unsigned WS_XG = 4096;
constexpr unsigned WS_QG = WS_XG + 64 * 4 * 2048;
constexpr unsigned WS_Q7 = WS_QG + 8 * 32768;
constexpr unsigned WS_F2 = WS_Q7 + 8 * 4096;
constexpr unsigned WS_X1 = WS_F2 + 256 * 1024;
constexpr unsigned WS_OG = WS_X1 + 256 * 2048;
constexpr unsigned WS_ST = WS_OG + 256 * 32768;
constexpr unsigned WS_END = WS_ST + 256 * 8 * 32 * 8;

constexpr unsigned L_T = 0;
constexpr unsigned L_WK = 3 * 32768;
constexpr unsigned L_CT = 131072;
constexpr unsigned L_MX = L_CT + 128;
constexpr unsigned L_QA = L_MX + 512;
constexpr unsigned L_SG = L_QA + 1024;
constexpr unsigned L_RED = L_SG;
constexpr unsigned L_QP = L_SG + 8192;
constexpr unsigned L_P = L_SG + 16384;
constexpr unsigned L_RED3 = L_SG + 16384;
constexpr unsigned L_LS = L_SG + 28 * 1024;
constexpr unsigned L_END = L_LS + 512;
enum { C_QP = 0, C_QB, C_QA, C_F1, C_QS, C_KV0, C_KV1, C_KV2, C_KV3, C_MX, C_P, C_LS,
       C_LAT, C_CTX, C_WST, C_WK, C_QST, C_X1, C_O3, C_PN, C_OL, C_N };

constexpr unsigned SENT = 0xFFFFFFFFu;
constexpr unsigned SPIN_CAP = 1u << 16;
constexpr unsigned LDS_CAP = 1u << 22;
constexpr float QSCALE = 0.08838834764831845f * 1.4426950408889634f;

struct Params {
  const void* x[2]; const void* ckv[2]; const void* wq[2]; const void* wuk[2]; const void* wuv[2]; const void* wo[2];
  void* out; void* ws;
};

DI f32x4 mfma16(v4u a, v4u b, f32x4 c) {
  return __builtin_amdgcn_mfma_f32_16x16x32_bf16(__builtin_bit_cast(bf16x8, a), __builtin_bit_cast(bf16x8, b), c, 0, 0, 0);
}
DI f32x16 mfma32(v4u a, v4u b, f32x16 c) {
  return __builtin_amdgcn_mfma_f32_32x32x16_bf16(__builtin_bit_cast(bf16x8, a), __builtin_bit_cast(bf16x8, b), c, 0, 0, 0);
}
typedef __bf16 bf2_t __attribute__((ext_vector_type(2)));
DI unsigned pk(float lo, float hi) {
  f32x2 f = {lo, hi};
  return __builtin_bit_cast(unsigned, __builtin_convertvector(f, bf2_t));
}
DI unsigned pk_ns(float lo, float hi) {
  const unsigned u = pk(lo, hi);
  return (u == SENT) ? 0x7FC07FC0u : u;
}
DI v4u tcol(const v4u (&w)[8], int i) {
  const unsigned sel = (i & 1) ? 0x07060302u : 0x05040100u;
  v4u o;
  o.x = __builtin_amdgcn_perm(w[1][i >> 1], w[0][i >> 1], sel);
  o.y = __builtin_amdgcn_perm(w[3][i >> 1], w[2][i >> 1], sel);
  o.z = __builtin_amdgcn_perm(w[5][i >> 1], w[4][i >> 1], sel);
  o.w = __builtin_amdgcn_perm(w[7][i >> 1], w[6][i >> 1], sel);
  return o;
}
#define CFENCE() asm volatile("" ::: "memory")
#define SB() __builtin_amdgcn_sched_barrier(0)
DI void lds_signal(LAS unsigned* c) {
  unsigned long long keep;
  asm volatile("s_mov_b64 %0, exec\n\ts_mov_b64 exec, 1\n\tds_add_u32 %1, %2\n\ts_mov_b64 exec, %0"
               : "=&s"(keep) : "v"((unsigned)(size_t)c), "v"(1u) : "memory");
}
DI unsigned lds_peek(LAS unsigned* c) { return (unsigned)__builtin_amdgcn_readfirstlane(__hip_atomic_load(c, RLX_WG)); }
DI void lds_wait_slow(LAS unsigned* c, unsigned target) {
  CFENCE();
  for (unsigned n = 0; n < LDS_CAP && lds_peek(c) < target; ++n) __builtin_amdgcn_s_sleep(1);
  CFENCE();
}
DI void lds_wait(LAS unsigned* c, unsigned target) {
  CFENCE();
  for (unsigned n = 0; n < LDS_CAP && lds_peek(c) < target; ++n) {}
  CFENCE();
}
DI float xor32_max(float v) {
  auto r = __builtin_amdgcn_permlane32_swap(__float_as_uint(v), __float_as_uint(v), false, false);
  return fmaxf(__uint_as_float(r[0]), __uint_as_float(r[1]));
}
DI float xor32_sum(float v) {
  auto r = __builtin_amdgcn_permlane32_swap(__float_as_uint(v), __float_as_uint(v), false, false);
  return __uint_as_float(r[0]) + __uint_as_float(r[1]);
}
DI float xor16_max(float v) {
  auto r = __builtin_amdgcn_permlane16_swap(__float_as_uint(v), __float_as_uint(v), false, false);
  return fmaxf(__uint_as_float(r[0]), __uint_as_float(r[1]));
}
DI float xor16_sum(float v) {
  auto r = __builtin_amdgcn_permlane16_swap(__float_as_uint(v), __float_as_uint(v), false, false);
  return __uint_as_float(r[0]) + __uint_as_float(r[1]);
}
template <int CTRL> DI float dpp_add(float v) {
  return v + __uint_as_float(__builtin_amdgcn_update_dpp(0u, __float_as_uint(v), CTRL, 0xF, 0xF, false));
}
DI void dma16(const void* sbase, unsigned voff, unsigned lds_dst) {
  unsigned keep;
  asm volatile("s_mov_b32 %0, m0\n\ts_mov_b32 m0, %3\n\ts_nop 0\n\tglobal_load_lds_dwordx4 %1, %2 nt\n\ts_mov_b32 m0, %0"
               : "=&s"(keep) : "v"(voff), "s"(sbase), "s"(lds_dst) : "memory");
}
template <int N> DI void wait_vm() { asm volatile("s_waitcnt vmcnt(%0)" ::"i"(N) : "memory"); }
#define STAMP(i) do {} while (0)
#define TS(i) do {} while (0)
#define TC(i) do {} while (0)
#define RSRC(ptr, bytes) __builtin_amdgcn_make_buffer_rsrc((void*)(ptr), 0, (int)(bytes), 0x00020000)
#define BLD(rs, vo, so, aux) __builtin_amdgcn_raw_buffer_load_b128(rs, (int)(vo), (int)(so), aux)
#define BLD64(rs, vo, so) __builtin_amdgcn_raw_buffer_load_b64(rs, (int)(vo), (int)(so), SC1)
#define BLD32(rs, vo, so) __builtin_amdgcn_raw_buffer_load_b32(rs, (int)(vo), (int)(so), SC1)
#define BST(v, rs, vo) __builtin_amdgcn_raw_buffer_store_b128(v, rs, (int)(vo), 0, SC1)
#define BST64(v, rs, vo) __builtin_amdgcn_raw_buffer_store_b64(v, rs, (int)(vo), 0, SC1)
#define BST32(v, rs, vo) __builtin_amdgcn_raw_buffer_store_b32(v, rs, (int)(vo), 0, SC1)
constexpr int SC1 = 16, NT = 2;

__global__ void __launch_bounds__(512, 2) mla_fused(Params P) {
  extern __shared__ __attribute__((aligned(16))) unsigned char smem[];
  LAS unsigned char* L = (LAS unsigned char*)smem;
  LAS unsigned* ctl = (LAS unsigned*)(L + L_CT);
  const int tid = threadIdx.x, lane = tid & 63;
  const int wave = __builtin_amdgcn_readfirstlane(tid >> 6);
  const int w = blockIdx.x;
  const int ph = w & 63, p1 = ph >> 5, h = w & 31, cq = w >> 6;
  const int src = h * 4 + cq;
  const int blk1 = p1 * 128 + src;
  const int pb = w & 7, p2 = pb >> 2, b2 = pb & 3, j = w >> 3;
  const unsigned oblk = p2 * 128 * 128 + b2 * 32 + j;
  const int r = lane & 15, g = lane >> 4;
  const void *a_x0 = P.x[0], *a_x1 = P.x[1], *a_kv0 = P.ckv[0], *a_kv1 = P.ckv[1], *a_q0 = P.wq[0], *a_q1 = P.wq[1], *a_k0 = P.wuk[0],
             *a_k1 = P.wuk[1], *a_v0 = P.wuv[0], *a_v1 = P.wuv[1], *a_o0 = P.wo[0], *a_o1 = P.wo[1];
  void *a_out = P.out, *a_ws = P.ws;
  asm volatile("" : "+s"(a_x0), "+s"(a_x1), "+s"(a_kv0), "+s"(a_kv1), "+s"(a_q0), "+s"(a_q1), "+s"(a_k0), "+s"(a_k1), "+s"(a_v0), "+s"(a_v1),
                    "+s"(a_o0), "+s"(a_o1), "+s"(a_out), "+s"(a_ws));
  const void* g_x = p1 ? a_x1 : a_x0;
  const void* g_kv = p2 ? a_kv1 : a_kv0;
  const void* g_wq = p1 ? a_q1 : a_q0;
  const void* g_wk = p1 ? a_k1 : a_k0;
  const void* g_wv = p1 ? a_v1 : a_v0;
  const void* g_wo = p1 ? a_o1 : a_o0;
  const __amdgpu_buffer_rsrc_t ws = RSRC(a_ws, WS_END);
  TS(0);
  const unsigned xo = WS_XG + ph * 8192 + tid * 8;
  const unsigned f1own = WS_F1 + ((p1 * 4 + (lane & 3)) * 128 + src) * 4;
  unsigned xtag = 0u, xs[3] = {0u, 0u, 0u}, f1tag = 0u;
  v2u f1s = {0u, 0u};
  v4u xf, wq[8];
  if (wave < 4) {
    const int aw = wave;
    const __amdgpu_buffer_rsrc_t rx = RSRC(g_x, 4 * 512 * 2);
    const __amdgpu_buffer_rsrc_t rq = RSRC(g_wq, 512 * 4096 * 2);
    xtag = BLD32(ws, xo + cq * 2048 + 4, 0);
#pragma unroll
    for (int k = 0; k < 3; ++k) xs[k] = BLD32(ws, xo + 4, ((cq + 1 + k) & 3) * 2048);
    f1s = BLD64(ws, WS_F1 + pb * 512 + lane * 8, 0);
    f1tag = BLD32(ws, f1own, 0);
    SB();
#pragma unroll
    for (int jj = 0; jj < 8; ++jj) wq[jj] = BLD(rq, g * 8 * 8192 + h * 256 + r * 16, (128 * cq + 32 * aw + jj) * 8192, NT);
    xf = BLD(rx, (r >> 2) * 1024 + g * 16, (128 * cq + 32 * aw) * 2, 0);
    SB();
  }
  if (tid < C_N) ctl[tid] = 0u;
  asm volatile("s_waitcnt lgkmcnt(0)" ::: "memory");
  __builtin_amdgcn_s_barrier();

  if (wave < 4) {
    const int aw = wave;
    __builtin_amdgcn_s_setprio(XPRIO);
    TS(1);
    unsigned xval;
    {
      f32x4 acc[8];
#pragma unroll
      for (int i = 0; i < 8; ++i) acc[i] = mfma16(xf, tcol(wq, i), f32x4{0.f, 0.f, 0.f, 0.f});
      *(LAS f32x4*)(L + L_RED + (aw * 4 + g) * 512 + r * 32) = f32x4{acc[0][0], acc[1][0], acc[2][0], acc[3][0]};
      *(LAS f32x4*)(L + L_RED + (aw * 4 + g) * 512 + r * 32 + 16) = f32x4{acc[4][0], acc[5][0], acc[6][0], acc[7][0]};
      lds_signal(ctl + C_QP);
      lds_wait(ctl + C_QP, 4);
      f32x2 s = *(const LAS f32x2*)(L + L_RED + aw * 512 + lane * 8);
#pragma unroll
      for (int k = 1; k < 4; ++k) s += *(const LAS f32x2*)(L + L_RED + (k * 4 + aw) * 512 + lane * 8);
      xval = pk(s[0], s[1]);
    }
    xtag += 1u;
    BST64((v2u{xval, xtag}), ws, xo + cq * 2048);
    *(LAS unsigned*)(L + L_QP + (cq * 4 + aw) * 272 + lane * 4) = xval;
    TS(2);
#define HEARTBEAT(flags_too) do { xtag += 1u; BST64((v2u{xval, xtag}), ws, xo + cq * 2048); \
      if ((flags_too) && aw == 0) { f1tag += 1u; if (lane < 4) BST32(f1tag, ws, f1own); } } while (0)
    {
      v2u xv[3];
      __builtin_amdgcn_s_sleep(XS0);
      for (unsigned spins = 0; spins < SPIN_CAP; ++spins) {
        CFENCE();
#pragma unroll
        for (int k = 0; k < 3; ++k) xv[k] = BLD64(ws, xo, ((cq + 1 + k) & 3) * 2048);
        if (__all(xv[0].y != xs[0] && xv[1].y != xs[1] && xv[2].y != xs[2])) break;
        if ((spins & 3u) == 3u) HEARTBEAT(false);
        __builtin_amdgcn_s_sleep(XS);
      }
#pragma unroll
      for (int k = 0; k < 3; ++k) *(LAS unsigned*)(L + L_QP + (((cq + 1 + k) & 3) * 4 + aw) * 272 + lane * 4) = xv[k].x;
    }
    lds_signal(ctl + C_QB);
    TS(16);
    {
      lds_wait(ctl + C_WK, 1);
      v4u wk[2][4];
#pragma unroll
      for (int t = 0; t < 2; ++t)
#pragma unroll
        for (int s = 0; s < 4; ++s) wk[t][s] = *(const LAS v4u*)(L + L_WK + ((aw * 2 + t) * 4 + s) * 1024 + lane * 16);
      lds_wait(ctl + C_QB, 4);
      f32x4 a2[2] = {f32x4{0.f, 0.f, 0.f, 0.f}, f32x4{0.f, 0.f, 0.f, 0.f}};
#pragma unroll
      for (int s = 0; s < 4; ++s) {
        const v4u qf = *(const LAS v4u*)(L + L_QP + ((r & 3) * 4 + (r >> 2)) * 272 + s * 64 + g * 16);
        a2[0] = mfma16(qf, wk[0][s], a2[0]);
        a2[1] = mfma16(qf, wk[1][s], a2[1]);
      }
#pragma unroll
      for (int t = 0; t < 2; ++t) {
        const float v = ((a2[t][0] + a2[t][1]) + (a2[t][2] + a2[t][3])) * QSCALE;
        *(LAS unsigned short*)(L + L_QA + g * 256 + (32 * aw + 16 * t + r) * 2) = (unsigned short)(pk(v, v) & 0xFFFFu);
      }
    }
    lds_signal(ctl + C_QA);
    if (aw == 0) {
      lds_wait(ctl + C_QA, 5);
      const v4u v = *(const LAS v4u*)(L + L_QA + lane * 16);
      BST(v, ws, (cq == 3 && r >= 8) ? WS_Q7 + (((p1 * 4 + g) * 4 + ((r - 8) >> 1)) * 64 + (r & 1) * 32 + h) * 16
                                     : WS_QG + ((p1 * 4 + g) * 32 + h) * 1024 + cq * 256 + r * 16);
      lds_signal(ctl + C_QST);
      TS(14);
      wait_vm<0>();
      f1tag += 1u;
      if (lane < 4) BST32(f1tag, ws, f1own);
      TS(3);
    }
    const int r32 = lane & 31, hi = lane >> 5;
    v4u ta[32];
    const unsigned tbase = L_T + (32 * aw + r32) * 256;
    const unsigned tswz = ((r32 & 3) << 2) | ((r32 >> 2) & 3);
#define TA_LOAD(q) do { _Pragma("unroll") for (int s8 = 0; s8 < 8; ++s8) \
      ta[8 * (q) + s8] = *(const LAS v4u*)(L + tbase + (q) * 32768 + (((2 * s8 + hi) ^ tswz) << 4)); SB(); } while (0)
    lds_wait_slow(ctl + C_KV0, 1);
    TA_LOAD(0);
    lds_wait_slow(ctl + C_KV1, 1);
    TA_LOAD(1);
    if (aw == 0) {
      bool c0 = false, c1 = false;
      lds_wait_slow(ctl + C_PN, 1);
      for (unsigned spins = 0; spins < SPIN_CAP; ++spins) {
        CFENCE();
        const v2u f = BLD64(ws, WS_F1 + pb * 512 + lane * 8, 0);
        c0 = c0 || f.x != f1s.x; c1 = c1 || f.y != f1s.y;
        if (__all(c0 && c1)) break;
        if ((spins & 1u) == 1u) HEARTBEAT(true);
        __builtin_amdgcn_s_sleep(FS);
      }
      lds_signal(ctl + C_F1);
    } else {
      CFENCE();
      for (unsigned spins = 0; spins < LDS_CAP && lds_peek(ctl + C_F1) < 1u; ++spins) {
        __builtin_amdgcn_s_sleep(1);
        if ((spins & 127u) == 127u) HEARTBEAT(false);
      }
      CFENCE();
    }
    TS(4);
#define QRD(s) (*(const LAS v4u*)(L + L_SG + ((s) >> 2) * 4096 + r32 * 128 + (((2 * ((s) & 3) + hi) ^ ((r32 >> 1) & 7)) << 4)))
    v4u q7[4];
    {
      const int qrow = 8 * aw + (lane >> 3);
      v4u sg[7];
#pragma unroll
      for (int s = 0; s < 7; ++s) sg[s] = BLD(ws, qrow * 1024 + (lane & 7) * 16 + s * 128, WS_QG + pb * 32768, SC1);
#pragma unroll
      for (int s = 0; s < 4; ++s) q7[s] = BLD(ws, lane * 16 + s * 1024, WS_Q7 + pb * 4096, SC1);
      SB();
      lds_wait_slow(ctl + C_KV2, 1);
      TA_LOAD(2);
#pragma unroll
      for (int s = 0; s < 7; ++s) *(LAS v4u*)(L + L_SG + s * 4096 + qrow * 128 + (((lane & 7) ^ ((qrow >> 1) & 7)) << 4)) = sg[s];
      lds_signal(ctl + C_QS);
    }
    TS(5);
    lds_wait(ctl + C_KV3, 1);
    TA_LOAD(3);
    TS(6);
    lds_wait(ctl + C_QS, 4);
    TS(17);
    s16x4 tf[8][4][2];
    const int q4 = (lane & 15) >> 2, pp = lane & 3, blk = (lane >> 4) & 1;
#define TR_LOAD(ks, ct) do { _Pragma("unroll") for (int t = 0; t < 2; ++t) \
      tf[ks][ct][t] = __builtin_amdgcn_ds_read_tr16_b64_v4i16((LAS s16x4*)(L + L_T + aw * 32768 + (ks) * 4096 + (8 * t + 4 * hi + q4) * 256 + \
          (((ct) ^ q4) << 6) + (((2 * blk + (pp >> 1)) ^ ((2 * t + hi) & 3)) << 4) + ((pp & 1) << 3))); } while (0)
    f32x16 sa;
#pragma unroll
    for (int i = 0; i < 16; ++i) sa[i] = 0.f;
    {
      v4u qf[4];
#pragma unroll
      for (int s = 0; s < 4; ++s) qf[s] = QRD(s);
      SB();
#pragma unroll
      for (int s = 0; s < 32; ++s) {
        sa = mfma32(ta[s], s < 28 ? qf[s & 3] : q7[s & 3], sa);
        if (s + 4 < 28) qf[s & 3] = QRD(s + 4);
        if (s >= 1) TR_LOAD(((s - 1) >> 1) & 7, ((s - 1) >> 4) * 2 + ((s - 1) & 1));
        SB();
      }
      TR_LOAD(7, 3);
    }
    TC(22);
    float mx = sa[0];
#pragma unroll
    for (int i = 1; i < 16; ++i) mx = fmaxf(mx, sa[i]);
    mx = xor32_max(mx);
    *(LAS float*)(L + L_MX + r32 * 16 + aw * 4) = mx;
    lds_signal(ctl + C_MX);
    TC(23);
    lds_wait(ctl + C_MX, 4);
    TC(24);
    {
      const f32x4 m4 = *(const LAS f32x4*)(L + L_MX + r32 * 16);
      mx = fmaxf(fmaxf(m4[0], m4[1]), fmaxf(m4[2], m4[3]));
    }
    {
#pragma unroll
      for (int i = 0; i < 16; ++i) sa[i] = __builtin_amdgcn_exp2f(sa[i] - mx);
      *(LAS v4u*)(L + L_P + (2 * aw) * 1024 + lane * 16) = v4u{pk(sa[0], sa[1]), pk(sa[2], sa[3]), pk(sa[4], sa[5]), pk(sa[6], sa[7])};
      *(LAS v4u*)(L + L_P + (2 * aw + 1) * 1024 + lane * 16) = v4u{pk(sa[8], sa[9]), pk(sa[10], sa[11]), pk(sa[12], sa[13]), pk(sa[14], sa[15])};
      lds_signal(ctl + C_P);
      SB();
      float ps = (((sa[0] + sa[1]) + (sa[2] + sa[3])) + ((sa[4] + sa[5]) + (sa[6] + sa[7]))) +
                 (((sa[8] + sa[9]) + (sa[10] + sa[11])) + ((sa[12] + sa[13]) + (sa[14] + sa[15])));
      ps = xor32_sum(ps);
      *(LAS float*)(L + L_LS + r32 * 16 + aw * 4) = ps;
      lds_signal(ctl + C_LS);
    }
    TC(25);
    lds_wait(ctl + C_P, 4);
    TC(26);
    TS(7);
    v4u pf[8];
#pragma unroll
    for (int ks = 0; ks < 8; ++ks) pf[ks] = *(const LAS v4u*)(L + L_P + ks * 1024 + lane * 16);
    f32x16 oa[4];
#pragma unroll
    for (int ct = 0; ct < 4; ++ct)
#pragma unroll
      for (int i = 0; i < 16; ++i) oa[ct][i] = 0.f;
#define PV_MFMA(ks, ct) oa[ct] = mfma32(__builtin_bit_cast(v4u, __builtin_shufflevector(tf[ks][ct][0], tf[ks][ct][1], 0, 1, 2, 3, 4, 5, 6, 7)), pf[ks], oa[ct])
#define O_WRITE(half, ct, gg) *(LAS v2u*)(L + L_T + aw * 32768 + (half) * 4096 + r32 * 128 + (((4 * ((ct) & 1) + (gg)) ^ (r32 & 7)) << 4) + hi * 8) = \
      v2u{pk(oa[ct][4 * (gg)], oa[ct][4 * (gg) + 1]), pk(oa[ct][4 * (gg) + 2], oa[ct][4 * (gg) + 3])}
    const unsigned ord = L_T + aw * 32768 + (lane >> 3) * 128 + (((lane & 7) ^ ((lane >> 3) & 7)) << 4);
    const unsigned ost = WS_OG + (oblk + ((lane >> 3) * 4 + aw) * 128) * 256 + (lane & 7) * 16;
#pragma unroll
    for (int ks = 0; ks < 8; ++ks) { PV_MFMA(ks, 0); PV_MFMA(ks, 1); }
    SB();
    {
      v4u ov[4];
#pragma unroll
      for (int ks = 0; ks < 8; ++ks) {
        PV_MFMA(ks, 2);
        if (ks >= 1 && ks <= 4) { O_WRITE(0, 0, ks - 1); }
        SB();
        PV_MFMA(ks, 3);
        if (ks >= 1 && ks <= 4) { O_WRITE(0, 1, ks - 1); }
        if (ks == 5) {
          CFENCE();
#pragma unroll
          for (int u = 0; u < 4; ++u) ov[u] = *(const LAS v4u*)(L + ord + u * 1024);
        }
        if (ks == 7) {
#pragma unroll
          for (int u = 0; u < 4; ++u) __builtin_amdgcn_raw_buffer_store_b128(ov[u], ws, (int)ost, u * 32 * 32768, SC1);
        }
        SB();
      }
    }
    TC(27);
    TS(8);
#pragma unroll
    for (int gg = 0; gg < 4; ++gg) { O_WRITE(1, 2, gg); O_WRITE(1, 3, gg); }
    CFENCE();
    {
      v4u ov[4];
#pragma unroll
      for (int u = 0; u < 4; ++u) ov[u] = *(const LAS v4u*)(L + ord + 4096 + u * 1024);
#pragma unroll
      for (int u = 0; u < 4; ++u) __builtin_amdgcn_raw_buffer_store_b128(ov[u], ws, (int)(ost + 128), u * 32 * 32768, SC1);
    }
    TS(15);
    TC(29);
    {
      lds_wait(ctl + C_LS, 4);
      const f32x4 l4 = *(const LAS f32x4*)(L + L_LS + r32 * 16);
      const float lsum = (l4[0] + l4[1]) + (l4[2] + l4[3]);
      wait_vm<0>();
      if (hi == 0) BST64((v2u{__float_as_uint(mx), __float_as_uint(lsum) | 1u}), ws, WS_F2 + (oblk + (r32 * 4 + aw) * 128) * 8);
    }
    TC(30);
    TS(9);
    {
      v4u st[4];
      __builtin_amdgcn_s_sleep(DS0);
      const unsigned fo = WS_F2 + blk1 * 1024 + aw * 256 + g * 64;
      for (unsigned spins = 0; spins < SPIN_CAP; ++spins) {
        CFENCE();
#pragma unroll
        for (int k = 0; k < 4; ++k) st[k] = BLD(ws, fo + k * 16, 0, SC1);
        bool ok = true;
#pragma unroll
        for (int k = 0; k < 4; ++k) ok = ok && (st[k].y != 0u) && (st[k].w != 0u);
        if (__all(ok)) break;
        if ((spins & 7u) == 7u) HEARTBEAT(true);
        __builtin_amdgcn_s_sleep(DS);
      }
      CFENCE();
      TS(10);
      v4u od[8];
#pragma unroll
      for (int i = 0; i < 8; ++i) od[i] = BLD(ws, (8 * g + i) * 256 + r * 16, WS_OG + blk1 * 32768 + aw * 8192, SC1);
      SB();
      lds_signal(ctl + C_OL);
      float m[8], l[8];
#pragma unroll
      for (int k = 0; k < 4; ++k) {
        m[2 * k] = __uint_as_float(st[k].x); l[2 * k] = __uint_as_float(st[k].y);
        m[2 * k + 1] = __uint_as_float(st[k].z); l[2 * k + 1] = __uint_as_float(st[k].w);
      }
      float M = m[0];
#pragma unroll
      for (int i = 1; i < 8; ++i) M = fmaxf(M, m[i]);
      M = xor32_max(xor16_max(M));
      float Ls = 0.f;
#pragma unroll
      for (int i = 0; i < 8; ++i) { m[i] = __builtin_amdgcn_exp2f(m[i] - M); Ls += m[i] * l[i]; }
      Ls = xor32_sum(xor16_sum(Ls));
      const float inv = 1.0f / Ls;
      v4u wf;
#pragma unroll
      for (int k = 0; k < 4; ++k) {
        const float w0 = m[2 * k] * inv, w1 = m[2 * k + 1] * inv;
        const unsigned hi2 = pk(w0, w1);
        const unsigned lo2 = pk(w0 - __uint_as_float(hi2 << 16), w1 - __uint_as_float(hi2 & 0xFFFF0000u));
        wf[k] = (r & 1) ? lo2 : hi2;
      }
      SB();
      f32x4 la[8];
#pragma unroll
      for (int e = 0; e < 8; ++e) la[e] = mfma16(wf, tcol(od, e), f32x4{0.f, 0.f, 0.f, 0.f});
      const v4u o = {pk(la[0][0] + la[0][1], la[1][0] + la[1][1]), pk(la[2][0] + la[2][1], la[3][0] + la[3][1]),
                     pk(la[4][0] + la[4][1], la[5][0] + la[5][1]), pk(la[6][0] + la[6][1], la[7][0] + la[7][1])};
      if (g == 0) *(LAS v4u*)(L + L_QA + aw * 256 + r * 16) = o;
      lds_signal(ctl + C_LAT);
      TS(11);
    }
    if (aw == 0) {
      const int eb = h & 3, em = 4 * ((h >> 2) & 1) + (h >> 3);
      lds_wait_slow(ctl + C_WST, 4);
      v4u pv;
      const unsigned po = WS_X1 + (p1 * 128 + (lane >> 1) * 4 + cq) * 2048 + 1024 + eb * 256 + em * 32 + (lane & 1) * 16;
      for (unsigned spins = 0; spins < SPIN_CAP; ++spins) {
        CFENCE();
        pv = BLD(ws, po, 0, SC1);
        if (__all(pv.x != SENT && pv.y != SENT && pv.z != SENT && pv.w != SENT)) break;
        __builtin_amdgcn_s_sleep(1);
      }
      TS(12);
      float s[8];
#pragma unroll
      for (int c = 0; c < 4; ++c) { s[2 * c] = __uint_as_float(pv[c] << 16); s[2 * c + 1] = __uint_as_float(pv[c] & 0xFFFF0000u); }
#pragma unroll
      for (int c = 0; c < 8; ++c) {
        float v = s[c];
        v = dpp_add<0x4E>(v);
        v = dpp_add<0x124>(v);
        v = dpp_add<0x128>(v);
        v = xor32_sum(xor16_sum(v));
        s[c] = v;
      }
      if (lane < 2) {
        const v4u o = {pk(s[0], s[1]), pk(s[2], s[3]), pk(s[4], s[5]), pk(s[6], s[7])};
        *(GAS v4u*)((char*)a_out + (eb * 1024 + p1 * 512 + cq * 128 + em * 16 + lane * 8) * 2) = o;
      }
      TS(13);
    }
  } else {
    const int lw = wave - 4;
    if (lw == 0) {
      const unsigned lds0 = (unsigned)(size_t)L;
      const unsigned long long tk0 = __builtin_amdgcn_s_memtime();
      __builtin_amdgcn_s_setprio(3);
      lds_wait(ctl + C_QP, 1);
      {
        const char* wkb = (const char*)g_wk + (size_t)(128 * cq) * 8192 + h * 256;
        const unsigned vk = r * 8192 + g * 16;
        for (int G = 0; G < 4; ++G) {
#pragma unroll
          for (int i = 0; i < 8; ++i) dma16(wkb + (size_t)(G * 2 + (i >> 2)) * 16 * 8192 + (i & 3) * 64, vk, lds0 + L_WK + (G * 8 + i) * 1024);
        }
        wait_vm<0>();
        lds_signal(ctl + C_WK);
        while (lds_peek(ctl + C_QST) < 1u && __builtin_amdgcn_s_memtime() - tk0 < (unsigned long long)TRES) __builtin_amdgcn_s_sleep(1);
        CFENCE();
      }
      const char* kvb = (const char*)g_kv + ((size_t)b2 * 4096 + 128 * j) * 1024;
      unsigned vo[4];
#pragma unroll
      for (int u3 = 0; u3 < 4; ++u3) vo[u3] = g * 1024 + ((r ^ ((g << 2) | u3)) << 4);
      for (int G = 0; G < 16; ++G) {
        wait_vm<KDEPTH - 8>();
        const int done = 8 * G - (KDEPTH - 8);
        if (G == 12) lds_wait_slow(ctl + C_QST, 1);
        if (G == PG) lds_signal(ctl + C_PN);
        if (done == 32) lds_signal(ctl + C_KV0);
        if (done == 64) lds_signal(ctl + C_KV1);
        if (done == 96) lds_signal(ctl + C_KV2);
        const int q = G >> 2, u0 = (G & 3) * 8;
        const char* gb = kvb + u0 * 4096 + q * 256;
        const unsigned lb = lds0 + L_T + q * 32768 + u0 * 1024;
#pragma unroll
        for (int i = 0; i < 8; ++i) dma16(gb + i * 4096, vo[i & 3], lb + i * 1024);
      }
      TS(1);
      if (120 - (KDEPTH - 8) < 96) { wait_vm<32>(); lds_signal(ctl + C_KV2); }
      wait_vm<0>();
      lds_signal(ctl + C_KV3);
      __builtin_amdgcn_s_setprio(0);
      TS(2);
    } else if (lw == 1) {
      if (j < CODEPF) {
        unsigned long long pc;
        asm volatile("s_getpc_b64 %0" : "=s"(pc));
        const GAS char* cb = (const GAS char*)((pc & ~0xFFull) - CODE_BACK) + lane * 128;
        unsigned acc = 0;
#pragma unroll
        for (int k = 0; k < CODE_KB / 8; ++k) acc += *(const volatile GAS unsigned*)(cb + k * 8192);
        asm volatile("" :: "v"(acc));
      }
    } else if (lw == 3) {
      {
        const v4u z = {0u, 0u, 0u, 0u}, sn = {SENT, SENT, SENT, SENT};
        BST(z, ws, WS_F2 + blk1 * 1024 + lane * 16);
        BST(sn, ws, WS_X1 + blk1 * 2048 + lane * 16);
        BST(sn, ws, WS_X1 + blk1 * 2048 + 1024 + lane * 16);
        wait_vm<0>();
        lds_signal(ctl + C_QA);
      }
    }
    const __amdgpu_buffer_rsrc_t rv = RSRC(g_wv, 512 * 4096 * 2);
    const __amdgpu_buffer_rsrc_t ro = RSRC(g_wo, 512 * 4096 * 2);
    v4u wv[8], wo[8];
    if (WISS) lds_wait_slow(ctl + C_KV3, 1);
    lds_wait_slow(ctl + C_QS, 4);
#pragma unroll
    for (int jj = 0; jj < 8; ++jj) wv[jj] = BLD(rv, g * 8 * 8192 + h * 256 + r * 16, (128 * cq + 32 * lw + jj) * 8192, NT);
    SB();
    TS(3);
    v4u tv[8], to[8];
#pragma unroll
    for (int i = 0; i < 8; ++i) { tv[i] = tcol(wv, i); asm volatile("" : "+v"(tv[i])); }
    lds_wait_slow(ctl + C_OL, 4);
#pragma unroll
    for (int jj = 0; jj < 8; ++jj) wo[jj] = BLD(ro, g * 8 * 1024 + r * 16, (h * 128 + 32 * lw + jj) * 1024 + cq * 256, NT);
    SB();
    TS(4);
    lds_wait_slow(ctl + C_LAT, 4);
    {
      const v4u lf = *(const LAS v4u*)(L + L_QA + (r >> 2) * 256 + lw * 64 + g * 16);
      f32x4 ca[8];
#pragma unroll
      for (int i = 0; i < 8; ++i) ca[i] = mfma16(lf, tv[i], f32x4{0.f, 0.f, 0.f, 0.f});
      *(LAS f32x4*)(L + L_RED + (lw * 4 + g) * 512 + r * 32) = f32x4{ca[0][0], ca[1][0], ca[2][0], ca[3][0]};
      *(LAS f32x4*)(L + L_RED + (lw * 4 + g) * 512 + r * 32 + 16) = f32x4{ca[4][0], ca[5][0], ca[6][0], ca[7][0]};
      lds_signal(ctl + C_CTX);
      lds_wait(ctl + C_CTX, 4);
      f32x2 s = *(const LAS f32x2*)(L + L_RED + lw * 512 + lane * 8);
#pragma unroll
      for (int k = 1; k < 4; ++k) s += *(const LAS f32x2*)(L + L_RED + (k * 4 + lw) * 512 + lane * 8);
      const unsigned cv = pk_ns(s[0], s[1]);
      BST32(cv, ws, WS_X1 + blk1 * 2048 + lw * 256 + lane * 4);
      *(LAS unsigned*)(L + L_QP + (cq * 4 + lw) * 272 + lane * 4) = cv;
    }
    TS(5);
#pragma unroll
    for (int i = 0; i < 8; ++i) { to[i] = tcol(wo, i); asm volatile("" : "+v"(to[i])); }
    {
      unsigned xv[3];
      const unsigned xb = WS_X1 + (p1 * 128 + h * 4) * 2048 + lw * 256 + lane * 4;
      for (unsigned spins = 0; spins < SPIN_CAP; ++spins) {
        CFENCE();
#pragma unroll
        for (int k = 0; k < 3; ++k) xv[k] = BLD32(ws, xb, ((cq + 1 + k) & 3) * 2048);
        if (__all(xv[0] != SENT && xv[1] != SENT && xv[2] != SENT)) break;
        __builtin_amdgcn_s_sleep(1);
      }
#pragma unroll
      for (int k = 0; k < 3; ++k) *(LAS unsigned*)(L + L_QP + (((cq + 1 + k) & 3) * 4 + lw) * 272 + lane * 4) = xv[k];
    }
    lds_signal(ctl + C_X1);
    TS(7);
    lds_wait(ctl + C_X1, 4);
    {
      const v4u cf = *(const LAS v4u*)(L + L_QP + ((r & 3) * 4 + (r >> 2)) * 272 + lw * 64 + g * 16);
      float o[8];
#pragma unroll
      for (int i = 0; i < 8; ++i) {
        const f32x4 a = mfma16(cf, to[i], f32x4{0.f, 0.f, 0.f, 0.f});
        o[i] = (a[0] + a[1]) + (a[2] + a[3]);
      }
      *(LAS f32x4*)(L + L_RED3 + (lw * 4 + g) * 512 + r * 32) = f32x4{o[0], o[1], o[2], o[3]};
      *(LAS f32x4*)(L + L_RED3 + (lw * 4 + g) * 512 + r * 32 + 16) = f32x4{o[4], o[5], o[6], o[7]};
      lds_signal(ctl + C_O3);
      lds_wait(ctl + C_O3, 4);
      f32x2 s = *(const LAS f32x2*)(L + L_RED3 + lw * 512 + lane * 8);
#pragma unroll
      for (int k = 1; k < 4; ++k) s += *(const LAS f32x2*)(L + L_RED3 + (k * 4 + lw) * 512 + lane * 8);
      BST32(pk_ns(s[0], s[1]), ws, WS_X1 + blk1 * 2048 + 1024 + lw * 256 + lane * 4);
    }
    lds_signal(ctl + C_WST);
    TS(6);
  }
}

extern "C" void kernel_launch(void* const* d_in, const int* in_sizes, int n_in, void* d_out, int out_size, void* d_ws,
                              size_t ws_size, hipStream_t stream) {
  Params P;
  for (int p = 0; p < 2; ++p) {
    P.x[p] = d_in[6 * p + 0]; P.ckv[p] = d_in[6 * p + 1]; P.wq[p] = d_in[6 * p + 2];
    P.wuk[p] = d_in[6 * p + 3]; P.wuv[p] = d_in[6 * p + 4]; P.wo[p] = d_in[6 * p + 5];
  }
  P.out = d_out; P.ws = d_ws;
  (void)hipFuncSetAttribute((const void*)mla_fused, hipFuncAttributeMaxDynamicSharedMemorySize, (int)L_END);
  mla_fused<<<256, 512, L_END, stream>>>(P);
}
```

```cpp
#include <hip/hip_runtime.h>
#include <hip/hip_bf16.h>

#define GAS __attribute__((address_space(1)))
#define LAS __attribute__((address_space(3)))
typedef unsigned v4u __attribute__((ext_vector_type(4)));
typedef unsigned v2u __attribute__((ext_vector_type(2)));
typedef float f32x2 __attribute__((ext_vector_type(2)));
typedef float f32x4 __attribute__((ext_vector_type(4)));
typedef float f32x16 __attribute__((ext_vector_type(16)));
typedef short bf16x8 __attribute__((ext_vector_type(8)));
typedef short s16x4 __attribute__((ext_vector_type(4)));
#define RLX_WG __ATOMIC_RELAXED, __HIP_MEMORY_SCOPE_WORKGROUP
#define DI __device__ __forceinline__

#define KDEPTH 40
#define PG 12
#define XS0 24
#define XS 8
#define FS 16
#define DS0 16
#define DS 6
#define XPRIO 1
#define TRES 7000
#define WISS 1
#define CODEPF 0
#define CODE_BACK 0
#define CODE_KB 16
#define STAMPS 0
#define DELAY_TEST 0

constexpr unsigned WS_F1 = 0;
constexpr unsigned WS_XG = 4096;
constexpr unsigned WS_QG = WS_XG + 64 * 4 * 2048;
constexpr unsigned WS_Q7 = WS_QG + 8 * 32768;
constexpr unsigned WS_F2 = WS_Q7 + 8 * 4096;
constexpr unsigned WS_X1 = WS_F2 + 256 * 1024;
constexpr unsigned WS_OG = WS_X1 + 256 * 2048;
constexpr unsigned WS_ST = WS_OG + 256 * 32768;
constexpr unsigned WS_END = WS_ST + 256 * 8 * 32 * 8;

constexpr unsigned L_T = 0;
constexpr unsigned L_WK = 3 * 32768;
constexpr unsigned L_CT = 131072;
constexpr unsigned L_MX = L_CT + 128;
constexpr unsigned L_QA = L_MX + 512;
constexpr unsigned L_SG = L_QA + 1024;
constexpr unsigned L_RED = L_SG;
constexpr unsigned L_QP = L_SG + 8192;
constexpr unsigned L_P = L_SG + 16384;
constexpr unsigned L_RED3 = L_SG + 16384;
constexpr unsigned L_LS = L_SG + 28 * 1024;
constexpr unsigned L_END = L_LS + 512;
enum { C_QP = 0, C_QB, C_QA, C_F1, C_QS, C_KV0, C_KV1, C_KV2, C_KV3, C_MX, C_P, C_LS,
       C_LAT, C_CTX, C_WST, C_WK, C_QST, C_X1, C_O3, C_PN, C_OL, C_N };

constexpr unsigned SENT = 0xFFFFFFFFu;
constexpr unsigned SPIN_CAP = 1u << 16;
constexpr unsigned LDS_CAP = 1u << 22;
constexpr float QSCALE = 0.08838834764831845f * 1.4426950408889634f;

struct Params {
  const void* x[2]; const void* ckv[2]; const void* wq[2]; const void* wuk[2]; const void* wuv[2]; const void* wo[2];
  void* out; void* ws;
};

DI f32x4 mfma16(v4u a, v4u b, f32x4 c) {
  return __builtin_amdgcn_mfma_f32_16x16x32_bf16(__builtin_bit_cast(bf16x8, a), __builtin_bit_cast(bf16x8, b), c, 0, 0, 0);
}
DI f32x16 mfma32(v4u a, v4u b, f32x16 c) {
  return __builtin_amdgcn_mfma_f32_32x32x16_bf16(__builtin_bit_cast(bf16x8, a), __builtin_bit_cast(bf16x8, b), c, 0, 0, 0);
}
typedef __bf16 bf2_t __attribute__((ext_vector_type(2)));
DI unsigned pk(float lo, float hi) {
  f32x2 f = {lo, hi};
  return __builtin_bit_cast(unsigned, __builtin_convertvector(f, bf2_t));
}
DI unsigned pk_ns(float lo, float hi) {
  const unsigned u = pk(lo, hi);
  return (u == SENT) ? 0x7FC07FC0u : u;
}
DI v4u tcol(const v4u (&w)[8], int i) {
  const unsigned sel = (i & 1) ? 0x07060302u : 0x05040100u;
  v4u o;
  o.x = __builtin_amdgcn_perm(w[1][i >> 1], w[0][i >> 1], sel);
  o.y = __builtin_amdgcn_perm(w[3][i >> 1], w[2][i >> 1], sel);
  o.z = __builtin_amdgcn_perm(w[5][i >> 1], w[4][i >> 1], sel);
  o.w = __builtin_amdgcn_perm(w[7][i >> 1], w[6][i >> 1], sel);
  return o;
}
#define CFENCE() asm volatile("" ::: "memory")
#define SB() __builtin_amdgcn_sched_barrier(0)
DI void lds_signal(LAS unsigned* c) {
  unsigned long long keep;
  asm volatile("s_mov_b64 %0, exec\n\ts_mov_b64 exec, 1\n\tds_add_u32 %1, %2\n\ts_mov_b64 exec, %0"
               : "=&s"(keep) : "v"((unsigned)(size_t)c), "v"(1u) : "memory");
}
DI unsigned lds_peek(LAS unsigned* c) { return (unsigned)__builtin_amdgcn_readfirstlane(__hip_atomic_load(c, RLX_WG)); }
DI void lds_wait_slow(LAS unsigned* c, unsigned target) {
  CFENCE();
  for (unsigned n = 0; n < LDS_CAP && lds_peek(c) < target; ++n) __builtin_amdgcn_s_sleep(1);
  CFENCE();
}
DI void lds_wait(LAS unsigned* c, unsigned target) {
  CFENCE();
  for (unsigned n = 0; n < LDS_CAP && lds_peek(c) < target; ++n) {}
  CFENCE();
}
DI float xor32_max(float v) {
  auto r = __builtin_amdgcn_permlane32_swap(__float_as_uint(v), __float_as_uint(v), false, false);
  return fmaxf(__uint_as_float(r[0]), __uint_as_float(r[1]));
}
DI float xor32_sum(float v) {
  auto r = __builtin_amdgcn_permlane32_swap(__float_as_uint(v), __float_as_uint(v), false, false);
  return __uint_as_float(r[0]) + __uint_as_float(r[1]);
}
DI float xor16_max(float v) {
  auto r = __builtin_amdgcn_permlane16_swap(__float_as_uint(v), __float_as_uint(v), false, false);
  return fmaxf(__uint_as_float(r[0]), __uint_as_float(r[1]));
}
DI float xor16_sum(float v) {
  auto r = __builtin_amdgcn_permlane16_swap(__float_as_uint(v), __float_as_uint(v), false, false);
  return __uint_as_float(r[0]) + __uint_as_float(r[1]);
}
template <int CTRL> DI float dpp_add(float v) {
  return v + __uint_as_float(__builtin_amdgcn_update_dpp(0u, __float_as_uint(v), CTRL, 0xF, 0xF, false));
}
DI void dma16(const void* sbase, unsigned voff, unsigned lds_dst) {
  unsigned keep;
  asm volatile("s_mov_b32 %0, m0\n\ts_mov_b32 m0, %3\n\ts_nop 0\n\tglobal_load_lds_dwordx4 %1, %2 nt\n\ts_mov_b32 m0, %0"
               : "=&s"(keep) : "v"(voff), "s"(sbase), "s"(lds_dst) : "memory");
}
template <int N> DI void wait_vm() { asm volatile("s_waitcnt vmcnt(%0)" ::"i"(N) : "memory"); }
#define STAMP(i) do {} while (0)
#define TS(i) do {} while (0)
#define TC(i) do {} while (0)
#define RSRC(ptr, bytes) __builtin_amdgcn_make_buffer_rsrc((void*)(ptr), 0, (int)(bytes), 0x00020000)
#define BLD(rs, vo, so, aux) __builtin_amdgcn_raw_buffer_load_b128(rs, (int)(vo), (int)(so), aux)
#define BLD64(rs, vo, so) __builtin_amdgcn_raw_buffer_load_b64(rs, (int)(vo), (int)(so), SC1)
#define BLD32(rs, vo, so) __builtin_amdgcn_raw_buffer_load_b32(rs, (int)(vo), (int)(so), SC1)
#define BST(v, rs, vo) __builtin_amdgcn_raw_buffer_store_b128(v, rs, (int)(vo), 0, SC1)
#define BST64(v, rs, vo) __builtin_amdgcn_raw_buffer_store_b64(v, rs, (int)(vo), 0, SC1)
#define BST32(v, rs, vo) __builtin_amdgcn_raw_buffer_store_b32(v, rs, (int)(vo), 0, SC1)
constexpr int SC1 = 16, NT = 2;

__global__ void __launch_bounds__(512, 2) mla_fused(Params P) {
  extern __shared__ __attribute__((aligned(16))) unsigned char smem[];
  LAS unsigned char* L = (LAS unsigned char*)smem;
  LAS unsigned* ctl = (LAS unsigned*)(L + L_CT);
  const int tid = threadIdx.x, lane = tid & 63;
  const int wave = __builtin_amdgcn_readfirstlane(tid >> 6);
  const int w = blockIdx.x;
  const int ph = w & 63, p1 = ph >> 5, h = w & 31, cq = w >> 6;
  const int src = h * 4 + cq;
  const int blk1 = p1 * 128 + src;
  const int pb = w & 7, p2 = pb >> 2, b2 = pb & 3, j = w >> 3;
  const unsigned oblk = p2 * 128 * 128 + b2 * 32 + j;
  const int r = lane & 15, g = lane >> 4;
  const void *a_x0 = P.x[0], *a_x1 = P.x[1], *a_kv0 = P.ckv[0], *a_kv1 = P.ckv[1], *a_q0 = P.wq[0], *a_q1 = P.wq[1], *a_k0 = P.wuk[0],
             *a_k1 = P.wuk[1], *a_v0 = P.wuv[0], *a_v1 = P.wuv[1], *a_o0 = P.wo[0], *a_o1 = P.wo[1];
  void *a_out = P.out, *a_ws = P.ws;
  asm volatile("" : "+s"(a_x0), "+s"(a_x1), "+s"(a_kv0), "+s"(a_kv1), "+s"(a_q0), "+s"(a_q1), "+s"(a_k0), "+s"(a_k1), "+s"(a_v0), "+s"(a_v1),
                    "+s"(a_o0), "+s"(a_o1), "+s"(a_out), "+s"(a_ws));
  const void* g_x = p1 ? a_x1 : a_x0;
  const void* g_kv = p2 ? a_kv1 : a_kv0;
  const void* g_wq = p1 ? a_q1 : a_q0;
  const void* g_wk = p1 ? a_k1 : a_k0;
  const void* g_wv = p1 ? a_v1 : a_v0;
  const void* g_wo = p1 ? a_o1 : a_o0;
  const __amdgpu_buffer_rsrc_t ws = RSRC(a_ws, WS_END);
  TS(0);
  const unsigned xo = WS_XG + ph * 8192 + tid * 8;
  const unsigned f1own = WS_F1 + ((p1 * 4 + (lane & 3)) * 128 + src) * 4;
  unsigned xtag = 0u, xs[3] = {0u, 0u, 0u}, f1tag = 0u;
  v2u f1s = {0u, 0u};
  v4u xf, wq[8];
  if (wave < 4) {
    const int aw = wave;
    const __amdgpu_buffer_rsrc_t rx = RSRC(g_x, 4 * 512 * 2);
    const __amdgpu_buffer_rsrc_t rq = RSRC(g_wq, 512 * 4096 * 2);
    xtag = BLD32(ws, xo + cq * 2048 + 4, 0);
#pragma unroll
    for (int k = 0; k < 3; ++k) xs[k] = BLD32(ws, xo + 4, ((cq + 1 + k) & 3) * 2048);
    f1s = BLD64(ws, WS_F1 + pb * 512 + lane * 8, 0);
    f1tag = BLD32(ws, f1own, 0);
    SB();
#pragma unroll
    for (int jj = 0; jj < 8; ++jj) wq[jj] = BLD(rq, g * 8 * 8192 + h * 256 + r * 16, (128 * cq + 32 * aw + jj) * 8192, NT);
    xf = BLD(rx, (r >> 2) * 1024 + g * 16, (128 * cq + 32 * aw) * 2, 0);
    SB();
  }
  if (tid < C_N) ctl[tid] = 0u;
  asm volatile("s_waitcnt lgkmcnt(0)" ::: "memory");
  __builtin_amdgcn_s_barrier();

  if (wave < 4) {
    const int aw = wave;
    __builtin_amdgcn_s_setprio(XPRIO);
    TS(1);
    unsigned xval;
    {
      f32x4 acc[8];
#pragma unroll
      for (int i = 0; i < 8; ++i) acc[i] = mfma16(xf, tcol(wq, i), f32x4{0.f, 0.f, 0.f, 0.f});
      *(LAS f32x4*)(L + L_RED + (aw * 4 + g) * 512 + r * 32) = f32x4{acc[0][0], acc[1][0], acc[2][0], acc[3][0]};
      *(LAS f32x4*)(L + L_RED + (aw * 4 + g) * 512 + r * 32 + 16) = f32x4{acc[4][0], acc[5][0], acc[6][0], acc[7][0]};
      lds_signal(ctl + C_QP);
      lds_wait(ctl + C_QP, 4);
      f32x2 s = *(const LAS f32x2*)(L + L_RED + aw * 512 + lane * 8);
#pragma unroll
      for (int k = 1; k < 4; ++k) s += *(const LAS f32x2*)(L + L_RED + (k * 4 + aw) * 512 + lane * 8);
      xval = pk(s[0], s[1]);
    }
    xtag += 1u;
    BST64((v2u{xval, xtag}), ws, xo + cq * 2048);
    *(LAS unsigned*)(L + L_QP + (cq * 4 + aw) * 272 + lane * 4) = xval;
    TS(2);
#define HEARTBEAT(flags_too) do { xtag += 1u; BST64((v2u{xval, xtag}), ws, xo + cq * 2048); \
      if ((flags_too) && aw == 0) { f1tag += 1u; if (lane < 4) BST32(f1tag, ws, f1own); } } while (0)
    {
      v2u xv[3];
      __builtin_amdgcn_s_sleep(XS0);
      for (unsigned spins = 0; spins < SPIN_CAP; ++spins) {
        CFENCE();
#pragma unroll
        for (int k = 0; k < 3; ++k) xv[k] = BLD64(ws, xo, ((cq + 1 + k) & 3) * 2048);
        if (__all(xv[0].y != xs[0] && xv[1].y != xs[1] && xv[2].y != xs[2])) break;
        if ((spins & 3u) == 3u) HEARTBEAT(false);
        __builtin_amdgcn_s_sleep(XS);
      }
#pragma unroll
      for (int k = 0; k < 3; ++k) *(LAS unsigned*)(L + L_QP + (((cq + 1 + k) & 3) * 4 + aw) * 272 + lane * 4) = xv[k].x;
    }
    lds_signal(ctl + C_QB);
    TS(16);
    {
      lds_wait(ctl + C_WK, 1);
      v4u wk[2][4];
#pragma unroll
      for (int t = 0; t < 2; ++t)
#pragma unroll
        for (int s = 0; s < 4; ++s) wk[t][s] = *(const LAS v4u*)(L + L_WK + ((aw * 2 + t) * 4 + s) * 1024 + lane * 16);
      lds_wait(ctl + C_QB, 4);
      f32x4 a2[2] = {f32x4{0.f, 0.f, 0.f, 0.f}, f32x4{0.f, 0.f, 0.f, 0.f}};
#pragma unroll
      for (int s = 0; s < 4; ++s) {
        const v4u qf = *(const LAS v4u*)(L + L_QP + ((r & 3) * 4 + (r >> 2)) * 272 + s * 64 + g * 16);
        a2[0] = mfma16(qf, wk[0][s], a2[0]);
        a2[1] = mfma16(qf, wk[1][s], a2[1]);
      }
#pragma unroll
      for (int t = 0; t < 2; ++t) {
        const float v = ((a2[t][0] + a2[t][1]) + (a2[t][2] + a2[t][3])) * QSCALE;
        *(LAS unsigned short*)(L + L_QA + g * 256 + (32 * aw + 16 * t + r) * 2) = (unsigned short)(pk(v, v) & 0xFFFFu);
      }
    }
    lds_signal(ctl + C_QA);
    if (aw == 0) {
      lds_wait(ctl + C_QA, 5);
      const v4u v = *(const LAS v4u*)(L + L_QA + lane * 16);
      BST(v, ws, (cq == 3 && r >= 8) ? WS_Q7 + (((p1 * 4 + g) * 4 + ((r - 8) >> 1)) * 64 + (r & 1) * 32 + h) * 16
                                     : WS_QG + ((p1 * 4 + g) * 32 + h) * 1024 + cq * 256 + r * 16);
      lds_signal(ctl + C_QST);
      TS(14);
      wait_vm<0>();
      f1tag += 1u;
      if (lane < 4) BST32(f1tag, ws, f1own);
      TS(3);
    }
    const int r32 = lane & 31, hi = lane >> 5;
    v4u ta[32];
    const unsigned tbase = L_T + (32 * aw + r32) * 256;
    const unsigned tswz = ((r32 & 3) << 2) | ((r32 >> 2) & 3);
#define TA_LOAD(q) do { _Pragma("unroll") for (int s8 = 0; s8 < 8; ++s8) \
      ta[8 * (q) + s8] = *(const LAS v4u*)(L + tbase + (q) * 32768 + (((2 * s8 + hi) ^ tswz) << 4)); SB(); } while (0)
    lds_wait_slow(ctl + C_KV0, 1);
    TA_LOAD(0);
    lds_wait_slow(ctl + C_KV1, 1);
    TA_LOAD(1);
    if (aw == 0) {
      bool c0 = false, c1 = false;
      lds_wait_slow(ctl + C_PN, 1);
      for (unsigned spins = 0; spins < SPIN_CAP; ++spins) {
        CFENCE();
        const v2u f = BLD64(ws, WS_F1 + pb * 512 + lane * 8, 0);
        c0 = c0 || f.x != f1s.x; c1 = c1 || f.y != f1s.y;
        if (__all(c0 && c1)) break;
        if ((spins & 1u) == 1u) HEARTBEAT(true);
        __builtin_amdgcn_s_sleep(FS);
      }
      lds_signal(ctl + C_F1);
    } else {
      CFENCE();
      for (unsigned spins = 0; spins < LDS_CAP && lds_peek(ctl + C_F1) < 1u; ++spins) {
        __builtin_amdgcn_s_sleep(1);
        if ((spins & 127u) == 127u) HEARTBEAT(false);
      }
      CFENCE();
    }
    TS(4);
#define QRD(s) (*(const LAS v4u*)(L + L_SG + ((s) >> 2) * 4096 + r32 * 128 + (((2 * ((s) & 3) + hi) ^ ((r32 >> 1) & 7)) << 4)))
    v4u q7[4];
    {
      const int qrow = 8 * aw + (lane >> 3);
      v4u sg[7];
#pragma unroll
      for (int s = 0; s < 7; ++s) sg[s] = BLD(ws, qrow * 1024 + (lane & 7) * 16 + s * 128, WS_QG + pb * 32768, SC1);
#pragma unroll
      for (int s = 0; s < 4; ++s) q7[s] = BLD(ws, lane * 16 + s * 1024, WS_Q7 + pb * 4096, SC1);
      SB();
      lds_wait_slow(ctl + C_KV2, 1);
      TA_LOAD(2);
#pragma unroll
      for (int s = 0; s < 7; ++s) *(LAS v4u*)(L + L_SG + s * 4096 + qrow * 128 + (((lane & 7) ^ ((qrow >> 1) & 7)) << 4)) = sg[s];
      lds_signal(ctl + C_QS);
    }
    TS(5);
    lds_wait(ctl + C_KV3, 1);
    TA_LOAD(3);
    TS(6);
    lds_wait(ctl + C_QS, 4);
    TS(17);
    s16x4 tf[8][4][2];
    const int q4 = (lane & 15) >> 2, pp = lane & 3, blk = (lane >> 4) & 1;
#define TR_LOAD(ks, ct) do { _Pragma("unroll") for (int t = 0; t < 2; ++t) \
      tf[ks][ct][t] = __builtin_amdgcn_ds_read_tr16_b64_v4i16((LAS s16x4*)(L + L_T + aw * 32768 + (ks) * 4096 + (8 * t + 4 * hi + q4) * 256 + \
          (((ct) ^ q4) << 6) + (((2 * blk + (pp >> 1)) ^ ((2 * t + hi) & 3)) << 4) + ((pp & 1) << 3))); } while (0)
    f32x16 sa;
#pragma unroll
    for (int i = 0; i < 16; ++i) sa[i] = 0.f;
    {
      v4u qf[4];
#pragma unroll
      for (int s = 0; s < 4; ++s) qf[s] = QRD(s);
      SB();
#pragma unroll
      for (int s = 0; s < 32; ++s) {
        sa = mfma32(ta[s], s < 28 ? qf[s & 3] : q7[s & 3], sa);
        if (s + 4 < 28) qf[s & 3] = QRD(s + 4);
        if (s >= 1) TR_LOAD(((s - 1) >> 1) & 7, ((s - 1) >> 4) * 2 + ((s - 1) & 1));
        SB();
      }
      TR_LOAD(7, 3);
    }
    TC(22);
    float mx = sa[0];
#pragma unroll
    for (int i = 1; i < 16; ++i) mx = fmaxf(mx, sa[i]);
    mx = xor32_max(mx);
    *(LAS float*)(L + L_MX + r32 * 16 + aw * 4) = mx;
    lds_signal(ctl + C_MX);
    TC(23);
    lds_wait(ctl + C_MX, 4);
    TC(24);
    {
      const f32x4 m4 = *(const LAS f32x4*)(L + L_MX + r32 * 16);
      mx = fmaxf(fmaxf(m4[0], m4[1]), fmaxf(m4[2], m4[3]));
    }
    {
#pragma unroll
      for (int i = 0; i < 16; ++i) sa[i] = __builtin_amdgcn_exp2f(sa[i] - mx);
      *(LAS v4u*)(L + L_P + (2 * aw) * 1024 + lane * 16) = v4u{pk(sa[0], sa[1]), pk(sa[2], sa[3]), pk(sa[4], sa[5]), pk(sa[6], sa[7])};
      *(LAS v4u*)(L + L_P + (2 * aw + 1) * 1024 + lane * 16) = v4u{pk(sa[8], sa[9]), pk(sa[10], sa[11]), pk(sa[12], sa[13]), pk(sa[14], sa[15])};
      lds_signal(ctl + C_P);
      SB();
      float ps = (((sa[0] + sa[1]) + (sa[2] + sa[3])) + ((sa[4] + sa[5]) + (sa[6] + sa[7]))) +
                 (((sa[8] + sa[9]) + (sa[10] + sa[11])) + ((sa[12] + sa[13]) + (sa[14] + sa[15])));
      ps = xor32_sum(ps);
      *(LAS float*)(L + L_LS + r32 * 16 + aw * 4) = ps;
      lds_signal(ctl + C_LS);
    }
    TC(25);
    lds_wait(ctl + C_P, 4);
    TC(26);
    TS(7);
    v4u pf[8];
#pragma unroll
    for (int ks = 0; ks < 8; ++ks) pf[ks] = *(const LAS v4u*)(L + L_P + ks * 1024 + lane * 16);
    f32x16 oa[4];
#pragma unroll
    for (int ct = 0; ct < 4; ++ct)
#pragma unroll
      for (int i = 0; i < 16; ++i) oa[ct][i] = 0.f;
#define PV_MFMA(ks, ct) oa[ct] = mfma32(__builtin_bit_cast(v4u, __builtin_shufflevector(tf[ks][ct][0], tf[ks][ct][1], 0, 1, 2, 3, 4, 5, 6, 7)), pf[ks], oa[ct])
#define O_WRITE(half, ct, gg) *(LAS v2u*)(L + L_T + aw * 32768 + (half) * 4096 + r32 * 128 + (((4 * ((ct) & 1) + (gg)) ^ (r32 & 7)) << 4) + hi * 8) = \
      v2u{pk(oa[ct][4 * (gg)], oa[ct][4 * (gg) + 1]), pk(oa[ct][4 * (gg) + 2], oa[ct][4 * (gg) + 3])}
    const unsigned ord = L_T + aw * 32768 + (lane >> 3) * 128 + (((lane & 7) ^ ((lane >> 3) & 7)) << 4);
    const unsigned ost = WS_OG + (oblk + ((lane >> 3) * 4 + aw) * 128) * 256 + (lane & 7) * 16;
#pragma unroll
    for (int ks = 0; ks < 8; ++ks) { PV_MFMA(ks, 0); PV_MFMA(ks, 1); }
    SB();
    {
      v4u ov[4];
#pragma unroll
      for (int ks = 0; ks < 8; ++ks) {
        PV_MFMA(ks, 2);
        if (ks >= 1 && ks <= 4) { O_WRITE(0, 0, ks - 1); }
        SB();
        PV_MFMA(ks, 3);
        if (ks >= 1 && ks <= 4) { O_WRITE(0, 1, ks - 1); }
        if (ks == 5) {
          CFENCE();
#pragma unroll
          for (int u = 0; u < 4; ++u) ov[u] = *(const LAS v4u*)(L + ord + u * 1024);
        }
        if (ks == 7) {
#pragma unroll
          for (int u = 0; u < 4; ++u) __builtin_amdgcn_raw_buffer_store_b128(ov[u], ws, (int)ost, u * 32 * 32768, SC1);
        }
        SB();
      }
    }
    TC(27);
    TS(8);
#pragma unroll
    for (int gg = 0; gg < 4; ++gg) { O_WRITE(1, 2, gg); O_WRITE(1, 3, gg); }
    CFENCE();
    {
      v4u ov[4];
#pragma unroll
      for (int u = 0; u < 4; ++u) ov[u] = *(const LAS v4u*)(L + ord + 4096 + u * 1024);
#pragma unroll
      for (int u = 0; u < 4; ++u) __builtin_amdgcn_raw_buffer_store_b128(ov[u], ws, (int)(ost + 128), u * 32 * 32768, SC1);
    }
    TS(15);
    TC(29);
    {
      lds_wait(ctl + C_LS, 4);
      const f32x4 l4 = *(const LAS f32x4*)(L + L_LS + r32 * 16);
      const float lsum = (l4[0] + l4[1]) + (l4[2] + l4[3]);
      wait_vm<0>();
      if (hi == 0) BST64((v2u{__float_as_uint(mx), __float_as_uint(lsum) | 1u}), ws, WS_F2 + (oblk + (r32 * 4 + aw) * 128) * 8);
    }
    TC(30);
    TS(9);
    {
      v4u st[4];
      __builtin_amdgcn_s_sleep(DS0);
      const unsigned fo = WS_F2 + blk1 * 1024 + aw * 256 + g * 64;
      for (unsigned spins = 0; spins < SPIN_CAP; ++spins) {
        CFENCE();
#pragma unroll
        for (int k = 0; k < 4; ++k) st[k] = BLD(ws, fo + k * 16, 0, SC1);
        bool ok = true;
#pragma unroll
        for (int k = 0; k < 4; ++k) ok = ok && (st[k].y != 0u) && (st[k].w != 0u);
        if (__all(ok)) break;
        if ((spins & 7u) == 7u) HEARTBEAT(true);
        __builtin_amdgcn_s_sleep(DS);
      }
      CFENCE();
      TS(10);
      v4u od[8];
#pragma unroll
      for (int i = 0; i < 8; ++i) od[i] = BLD(ws, (8 * g + i) * 256 + r * 16, WS_OG + blk1 * 32768 + aw * 8192, SC1);
      SB();
      lds_signal(ctl + C_OL);
      float m[8], l[8];
#pragma unroll
      for (int k = 0; k < 4; ++k) {
        m[2 * k] = __uint_as_float(st[k].x); l[2 * k] = __uint_as_float(st[k].y);
        m[2 * k + 1] = __uint_as_float(st[k].z); l[2 * k + 1] = __uint_as_float(st[k].w);
      }
      float M = m[0];
#pragma unroll
      for (int i = 1; i < 8; ++i) M = fmaxf(M, m[i]);
      M = xor32_max(xor16_max(M));
      float Ls = 0.f;
#pragma unroll
      for (int i = 0; i < 8; ++i) { m[i] = __builtin_amdgcn_exp2f(m[i] - M); Ls += m[i] * l[i]; }
      Ls = xor32_sum(xor16_sum(Ls));
      const float inv = 1.0f / Ls;
      v4u wf;
#pragma unroll
      for (int k = 0; k < 4; ++k) {
        const float w0 = m[2 * k] * inv, w1 = m[2 * k + 1] * inv;
        const unsigned hi2 = pk(w0, w1);
        const unsigned lo2 = pk(w0 - __uint_as_float(hi2 << 16), w1 - __uint_as_float(hi2 & 0xFFFF0000u));
        wf[k] = (r & 1) ? lo2 : hi2;
      }
      SB();
      f32x4 la[8];
#pragma unroll
      for (int e = 0; e < 8; ++e) la[e] = mfma16(wf, tcol(od, e), f32x4{0.f, 0.f, 0.f, 0.f});
      const v4u o = {pk(la[0][0] + la[0][1], la[1][0] + la[1][1]), pk(la[2][0] + la[2][1], la[3][0] + la[3][1]),
                     pk(la[4][0] + la[4][1], la[5][0] + la[5][1]), pk(la[6][0] + la[6][1], la[7][0] + la[7][1])};
      if (g == 0) *(LAS v4u*)(L + L_QA + aw * 256 + r * 16) = o;
      lds_signal(ctl + C_LAT);
      TS(11);
    }
    if (aw == 0) {
      const int eb = h & 3, em = 4 * ((h >> 2) & 1) + (h >> 3);
      lds_wait_slow(ctl + C_WST, 4);
      v4u pv;
      const unsigned po = WS_X1 + (p1 * 128 + (lane >> 1) * 4 + cq) * 2048 + 1024 + eb * 256 + em * 32 + (lane & 1) * 16;
      for (unsigned spins = 0; spins < SPIN_CAP; ++spins) {
        CFENCE();
        pv = BLD(ws, po, 0, SC1);
        if (__all(pv.x != SENT && pv.y != SENT && pv.z != SENT && pv.w != SENT)) break;
        __builtin_amdgcn_s_sleep(1);
      }
      TS(12);
      float s[8];
#pragma unroll
      for (int c = 0; c < 4; ++c) { s[2 * c] = __uint_as_float(pv[c] << 16); s[2 * c + 1] = __uint_as_float(pv[c] & 0xFFFF0000u); }
#pragma unroll
      for (int c = 0; c < 8; ++c) {
        float v = s[c];
        v = dpp_add<0x4E>(v);
        v = dpp_add<0x124>(v);
        v = dpp_add<0x128>(v);
        v = xor32_sum(xor16_sum(v));
        s[c] = v;
      }
      if (lane < 2) {
        const v4u o = {pk(s[0], s[1]), pk(s[2], s[3]), pk(s[4], s[5]), pk(s[6], s[7])};
        *(GAS v4u*)((char*)a_out + (eb * 1024 + p1 * 512 + cq * 128 + em * 16 + lane * 8) * 2) = o;
      }
      TS(13);
    }
  } else {
    const int lw = wave - 4;
    if (lw == 0) {
      const unsigned lds0 = (unsigned)(size_t)L;
      const unsigned long long tk0 = __builtin_amdgcn_s_memtime();
      __builtin_amdgcn_s_setprio(3);
      lds_wait(ctl + C_QP, 1);
      {
        const char* wkb = (const char*)g_wk + (size_t)(128 * cq) * 8192 + h * 256;
        const unsigned vk = r * 8192 + g * 16;
        for (int G = 0; G < 4; ++G) {
#pragma unroll
          for (int i = 0; i < 8; ++i) dma16(wkb + (size_t)(G * 2 + (i >> 2)) * 16 * 8192 + (i & 3) * 64, vk, lds0 + L_WK + (G * 8 + i) * 1024);
        }
        wait_vm<0>();
        lds_signal(ctl + C_WK);
        while (lds_peek(ctl + C_QST) < 1u && __builtin_amdgcn_s_memtime() - tk0 < (unsigned long long)TRES) __builtin_amdgcn_s_sleep(1);
        CFENCE();
      }
      const char* kvb = (const char*)g_kv + ((size_t)b2 * 4096 + 128 * j) * 1024;
      unsigned vo[4];
#pragma unroll
      for (int u3 = 0; u3 < 4; ++u3) vo[u3] = g * 1024 + ((r ^ ((g << 2) | u3)) << 4);
      for (int G = 0; G < 16; ++G) {
        wait_vm<KDEPTH - 8>();
        const int done = 8 * G - (KDEPTH - 8);
        if (G == 12) lds_wait_slow(ctl + C_QST, 1);
        if (G == PG) lds_signal(ctl + C_PN);
        if (done == 32) lds_signal(ctl + C_KV0);
        if (done == 64) lds_signal(ctl + C_KV1);
        if (done == 96) lds_signal(ctl + C_KV2);
        const int q = G >> 2, u0 = (G & 3) * 8;
        const char* gb = kvb + u0 * 4096 + q * 256;
        const unsigned lb = lds0 + L_T + q * 32768 + u0 * 1024;
#pragma unroll
        for (int i = 0; i < 8; ++i) dma16(gb + i * 4096, vo[i & 3], lb + i * 1024);
      }
      TS(1);
      if (120 - (KDEPTH - 8) < 96) { wait_vm<32>(); lds_signal(ctl + C_KV2); }
      wait_vm<0>();
      lds_signal(ctl + C_KV3);
      __builtin_amdgcn_s_setprio(0);
      TS(2);
    } else if (lw == 1) {
    } else if (lw == 3) {
      {
        const v4u z = {0u, 0u, 0u, 0u}, sn = {SENT, SENT, SENT, SENT};
        BST(z, ws, WS_F2 + blk1 * 1024 + lane * 16);
        BST(sn, ws, WS_X1 + blk1 * 2048 + lane * 16);
        BST(sn, ws, WS_X1 + blk1 * 2048 + 1024 + lane * 16);
        wait_vm<0>();
        lds_signal(ctl + C_QA);
      }
    }
    const __amdgpu_buffer_rsrc_t rv = RSRC(g_wv, 512 * 4096 * 2);
    const __amdgpu_buffer_rsrc_t ro = RSRC(g_wo, 512 * 4096 * 2);
    v4u wv[8], wo[8];
    if (WISS) lds_wait_slow(ctl + C_KV3, 1);
    lds_wait_slow(ctl + C_QS, 4);
#pragma unroll
    for (int jj = 0; jj < 8; ++jj) wv[jj] = BLD(rv, g * 8 * 8192 + h * 256 + r * 16, (128 * cq + 32 * lw + jj) * 8192, NT);
    SB();
    TS(3);
    v4u tv[8], to[8];
#pragma unroll
    for (int i = 0; i < 8; ++i) { tv[i] = tcol(wv, i); asm volatile("" : "+v"(tv[i])); }
    lds_wait_slow(ctl + C_OL, 4);
#pragma unroll
    for (int jj = 0; jj < 8; ++jj) wo[jj] = BLD(ro, g * 8 * 1024 + r * 16, (h * 128 + 32 * lw + jj) * 1024 + cq * 256, NT);
    SB();
    TS(4);
    lds_wait_slow(ctl + C_LAT, 4);
    {
      const v4u lf = *(const LAS v4u*)(L + L_QA + (r >> 2) * 256 + lw * 64 + g * 16);
      f32x4 ca[8];
#pragma unroll
      for (int i = 0; i < 8; ++i) ca[i] = mfma16(lf, tv[i], f32x4{0.f, 0.f, 0.f, 0.f});
      *(LAS f32x4*)(L + L_RED + (lw * 4 + g) * 512 + r * 32) = f32x4{ca[0][0], ca[1][0], ca[2][0], ca[3][0]};
      *(LAS f32x4*)(L + L_RED + (lw * 4 + g) * 512 + r * 32 + 16) = f32x4{ca[4][0], ca[5][0], ca[6][0], ca[7][0]};
      lds_signal(ctl + C_CTX);
      lds_wait(ctl + C_CTX, 4);
      f32x2 s = *(const LAS f32x2*)(L + L_RED + lw * 512 + lane * 8);
#pragma unroll
      for (int k = 1; k < 4; ++k) s += *(const LAS f32x2*)(L + L_RED + (k * 4 + lw) * 512 + lane * 8);
      const unsigned cv = pk_ns(s[0], s[1]);
      BST32(cv, ws, WS_X1 + blk1 * 2048 + lw * 256 + lane * 4);
      *(LAS unsigned*)(L + L_QP + (cq * 4 + lw) * 272 + lane * 4) = cv;
    }
    TS(5);
#pragma unroll
    for (int i = 0; i < 8; ++i) { to[i] = tcol(wo, i); asm volatile("" : "+v"(to[i])); }
    {
      unsigned xv[3];
      const unsigned xb = WS_X1 + (p1 * 128 + h * 4) * 2048 + lw * 256 + lane * 4;
      for (unsigned spins = 0; spins < SPIN_CAP; ++spins) {
        CFENCE();
#pragma unroll
        for (int k = 0; k < 3; ++k) xv[k] = BLD32(ws, xb, ((cq + 1 + k) & 3) * 2048);
        if (__all(xv[0] != SENT && xv[1] != SENT && xv[2] != SENT)) break;
        __builtin_amdgcn_s_sleep(1);
      }
#pragma unroll
      for (int k = 0; k < 3; ++k) *(LAS unsigned*)(L + L_QP + (((cq + 1 + k) & 3) * 4 + lw) * 272 + lane * 4) = xv[k];
    }
    lds_signal(ctl + C_X1);
    TS(7);
    lds_wait(ctl + C_X1, 4);
    {
      const v4u cf = *(const LAS v4u*)(L + L_QP + ((r & 3) * 4 + (r >> 2)) * 272 + lw * 64 + g * 16);
      float o[8];
#pragma unroll
      for (int i = 0; i < 8; ++i) {
        const f32x4 a = mfma16(cf, to[i], f32x4{0.f, 0.f, 0.f, 0.f});
        o[i] = (a[0] + a[1]) + (a[2] + a[3]);
      }
      *(LAS f32x4*)(L + L_RED3 + (lw * 4 + g) * 512 + r * 32) = f32x4{o[0], o[1], o[2], o[3]};
      *(LAS f32x4*)(L + L_RED3 + (lw * 4 + g) * 512 + r * 32 + 16) = f32x4{o[4], o[5], o[6], o[7]};
      lds_signal(ctl + C_O3);
      lds_wait(ctl + C_O3, 4);
      f32x2 s = *(const LAS f32x2*)(L + L_RED3 + lw * 512 + lane * 8);
#pragma unroll
      for (int k = 1; k < 4; ++k) s += *(const LAS f32x2*)(L + L_RED3 + (k * 4 + lw) * 512 + lane * 8);
      BST32(pk_ns(s[0], s[1]), ws, WS_X1 + blk1 * 2048 + 1024 + lw * 256 + lane * 4);
    }
    lds_signal(ctl + C_WST);
    TS(6);
  }
}

extern "C" void kernel_launch(void* const* d_in, const int* in_sizes, int n_in, void* d_out, int out_size, void* d_ws,
                              size_t ws_size, hipStream_t stream) {
  Params P;
  for (int p = 0; p < 2; ++p) {
    P.x[p] = d_in[6 * p + 0]; P.ckv[p] = d_in[6 * p + 1]; P.wq[p] = d_in[6 * p + 2];
    P.wuk[p] = d_in[6 * p + 3]; P.wuv[p] = d_in[6 * p + 4]; P.wo[p] = d_in[6 * p + 5];
  }
  P.out = d_out; P.ws = d_ws;
  (void)hipFuncSetAttribute((const void*)mla_fused, hipFuncAttributeMaxDynamicSharedMemorySize, (int)L_END);
  mla_fused<<<256, 512, L_END, stream>>>(P);
}
```

```cpp
#include <hip/hip_runtime.h>
#include <hip/hip_bf16.h>

#define GAS __attribute__((address_space(1)))
#define LAS __attribute__((address_space(3)))
typedef unsigned v4u __attribute__((ext_vector_type(4)));
typedef unsigned v2u __attribute__((ext_vector_type(2)));
typedef float f32x2 __attribute__((ext_vector_type(2)));
typedef float f32x4 __attribute__((ext_vector_type(4)));
typedef float f32x16 __attribute__((ext_vector_type(16)));
typedef short bf16x8 __attribute__((ext_vector_type(8)));
typedef short s16x4 __attribute__((ext_vector_type(4)));
#define RLX_WG __ATOMIC_RELAXED, __HIP_MEMORY_SCOPE_WORKGROUP
#define DI __device__ __forceinline__

#define KDEPTH 40
#define PG 12
#define XS0 24
#define XS 8
#define FS 16
#define DS0 16
#define DS 6
#define XPRIO 1
#define TRES 7000
#define WISS 1
#define CODEPF 0
#define CODE_TAIL 20
#define PRETOUCH 1
#define STAMPS 0
#define DELAY_TEST 0

constexpr unsigned WS_F1 = 0;
constexpr unsigned WS_XG = 4096;
constexpr unsigned WS_QG = WS_XG + 64 * 4 * 2048;
constexpr unsigned WS_Q7 = WS_QG + 8 * 32768;
constexpr unsigned WS_F2 = WS_Q7 + 8 * 4096;
constexpr unsigned WS_X1 = WS_F2 + 256 * 1024;
constexpr unsigned WS_OG = WS_X1 + 256 * 2048;
constexpr unsigned WS_ST = WS_OG + 256 * 32768;
constexpr unsigned WS_END = WS_ST + 256 * 8 * 32 * 8;

constexpr unsigned L_T = 0;
constexpr unsigned L_WK = 3 * 32768;
constexpr unsigned L_CT = 131072;
constexpr unsigned L_MX = L_CT + 128;
constexpr unsigned L_QA = L_MX + 512;
constexpr unsigned L_SG = L_QA + 1024;
constexpr unsigned L_RED = L_SG;
constexpr unsigned L_QP = L_SG + 8192;
constexpr unsigned L_P = L_SG + 16384;
constexpr unsigned L_RED3 = L_SG + 16384;
constexpr unsigned L_LS = L_SG + 28 * 1024;
constexpr unsigned L_END = L_LS + 512;
enum { C_QP = 0, C_QB, C_QA, C_F1, C_QS, C_KV0, C_KV1, C_KV2, C_KV3, C_MX, C_P, C_LS,
       C_LAT, C_CTX, C_WST, C_WK, C_QST, C_X1, C_O3, C_PN, C_OL, C_N };

constexpr unsigned SENT = 0xFFFFFFFFu;
constexpr unsigned SPIN_CAP = 1u << 16;
constexpr unsigned LDS_CAP = 1u << 22;
constexpr float QSCALE = 0.08838834764831845f * 1.4426950408889634f;

struct Params {
  const void* x[2]; const void* ckv[2]; const void* wq[2]; const void* wuk[2]; const void* wuv[2]; const void* wo[2];
  void* out; void* ws;
};

DI f32x4 mfma16(v4u a, v4u b, f32x4 c) {
  return __builtin_amdgcn_mfma_f32_16x16x32_bf16(__builtin_bit_cast(bf16x8, a), __builtin_bit_cast(bf16x8, b), c, 0, 0, 0);
}
DI f32x16 mfma32(v4u a, v4u b, f32x16 c) {
  return __builtin_amdgcn_mfma_f32_32x32x16_bf16(__builtin_bit_cast(bf16x8, a), __builtin_bit_cast(bf16x8, b), c, 0, 0, 0);
}
typedef __bf16 bf2_t __attribute__((ext_vector_type(2)));
DI unsigned pk(float lo, float hi) {
  f32x2 f = {lo, hi};
  return __builtin_bit_cast(unsigned, __builtin_convertvector(f, bf2_t));
}
DI unsigned pk_ns(float lo, float hi) {
  const unsigned u = pk(lo, hi);
  return (u == SENT) ? 0x7FC07FC0u : u;
}
DI v4u tcol(const v4u (&w)[8], int i) {
  const unsigned sel = (i & 1) ? 0x07060302u : 0x05040100u;
  v4u o;
  o.x = __builtin_amdgcn_perm(w[1][i >> 1], w[0][i >> 1], sel);
  o.y = __builtin_amdgcn_perm(w[3][i >> 1], w[2][i >> 1], sel);
  o.z = __builtin_amdgcn_perm(w[5][i >> 1], w[4][i >> 1], sel);
  o.w = __builtin_amdgcn_perm(w[7][i >> 1], w[6][i >> 1], sel);
  return o;
}
#define CFENCE() asm volatile("" ::: "memory")
#define SB() __builtin_amdgcn_sched_barrier(0)
DI void lds_signal(LAS unsigned* c) {
  unsigned long long keep;
  asm volatile("s_mov_b64 %0, exec\n\ts_mov_b64 exec, 1\n\tds_add_u32 %1, %2\n\ts_mov_b64 exec, %0"
               : "=&s"(keep) : "v"((unsigned)(size_t)c), "v"(1u) : "memory");
}
DI unsigned lds_peek(LAS unsigned* c) { return (unsigned)__builtin_amdgcn_readfirstlane(__hip_atomic_load(c, RLX_WG)); }
DI void lds_wait_slow(LAS unsigned* c, unsigned target) {
  CFENCE();
  for (unsigned n = 0; n < LDS_CAP && lds_peek(c) < target; ++n) __builtin_amdgcn_s_sleep(1);
  CFENCE();
}
DI void lds_wait(LAS unsigned* c, unsigned target) {
  CFENCE();
  for (unsigned n = 0; n < LDS_CAP && lds_peek(c) < target; ++n) {}
  CFENCE();
}
DI float xor32_max(float v) {
  auto r = __builtin_amdgcn_permlane32_swap(__float_as_uint(v), __float_as_uint(v), false, false);
  return fmaxf(__uint_as_float(r[0]), __uint_as_float(r[1]));
}
DI float xor32_sum(float v) {
  auto r = __builtin_amdgcn_permlane32_swap(__float_as_uint(v), __float_as_uint(v), false, false);
  return __uint_as_float(r[0]) + __uint_as_float(r[1]);
}
DI float xor16_max(float v) {
  auto r = __builtin_amdgcn_permlane16_swap(__float_as_uint(v), __float_as_uint(v), false, false);
  return fmaxf(__uint_as_float(r[0]), __uint_as_float(r[1]));
}
DI float xor16_sum(float v) {
  auto r = __builtin_amdgcn_permlane16_swap(__float_as_uint(v), __float_as_uint(v), false, false);
  return __uint_as_float(r[0]) + __uint_as_float(r[1]);
}
template <int CTRL> DI float dpp_add(float v) {
  return v + __uint_as_float(__builtin_amdgcn_update_dpp(0u, __float_as_uint(v), CTRL, 0xF, 0xF, false));
}
DI void dma16(const void* sbase, unsigned voff, unsigned lds_dst) {
  unsigned keep;
  asm volatile("s_mov_b32 %0, m0\n\ts_mov_b32 m0, %3\n\ts_nop 0\n\tglobal_load_lds_dwordx4 %1, %2 nt\n\ts_mov_b32 m0, %0"
               : "=&s"(keep) : "v"(voff), "s"(sbase), "s"(lds_dst) : "memory");
}
template <int N> DI void wait_vm() { asm volatile("s_waitcnt vmcnt(%0)" ::"i"(N) : "memory"); }
#define STAMP(i) do {} while (0)
#define TS(i) do {} while (0)
#define TC(i) do {} while (0)
#define RSRC(ptr, bytes) __builtin_amdgcn_make_buffer_rsrc((void*)(ptr), 0, (int)(bytes), 0x00020000)
#define BLD(rs, vo, so, aux) __builtin_amdgcn_raw_buffer_load_b128(rs, (int)(vo), (int)(so), aux)
#define BLD64(rs, vo, so) __builtin_amdgcn_raw_buffer_load_b64(rs, (int)(vo), (int)(so), SC1)
#define BLD32(rs, vo, so) __builtin_amdgcn_raw_buffer_load_b32(rs, (int)(vo), (int)(so), SC1)
#define BST(v, rs, vo) __builtin_amdgcn_raw_buffer_store_b128(v, rs, (int)(vo), 0, SC1)
#define BST64(v, rs, vo) __builtin_amdgcn_raw_buffer_store_b64(v, rs, (int)(vo), 0, SC1)
#define BST32(v, rs, vo) __builtin_amdgcn_raw_buffer_store_b32(v, rs, (int)(vo), 0, SC1)
constexpr int SC1 = 16, NT = 2;

__global__ void __launch_bounds__(512, 2) mla_fused(Params P) {
  extern __shared__ __attribute__((aligned(16))) unsigned char smem[];
  LAS unsigned char* L = (LAS unsigned char*)smem;
  LAS unsigned* ctl = (LAS unsigned*)(L + L_CT);
  const int tid = threadIdx.x, lane = tid & 63;
  const int wave = __builtin_amdgcn_readfirstlane(tid >> 6);
  const int w = blockIdx.x;
  const int ph = w & 63, p1 = ph >> 5, h = w & 31, cq = w >> 6;
  const int src = h * 4 + cq;
  const int blk1 = p1 * 128 + src;
  const int pb = w & 7, p2 = pb >> 2, b2 = pb & 3, j = w >> 3;
  const unsigned oblk = p2 * 128 * 128 + b2 * 32 + j;
  const int r = lane & 15, g = lane >> 4;
  const void *a_x0 = P.x[0], *a_x1 = P.x[1], *a_kv0 = P.ckv[0], *a_kv1 = P.ckv[1], *a_q0 = P.wq[0], *a_q1 = P.wq[1], *a_k0 = P.wuk[0],
             *a_k1 = P.wuk[1], *a_v0 = P.wuv[0], *a_v1 = P.wuv[1], *a_o0 = P.wo[0], *a_o1 = P.wo[1];
  void *a_out = P.out, *a_ws = P.ws;
  asm volatile("" : "+s"(a_x0), "+s"(a_x1), "+s"(a_kv0), "+s"(a_kv1), "+s"(a_q0), "+s"(a_q1), "+s"(a_k0), "+s"(a_k1), "+s"(a_v0), "+s"(a_v1),
                    "+s"(a_o0), "+s"(a_o1), "+s"(a_out), "+s"(a_ws));
  const void* g_x = p1 ? a_x1 : a_x0;
  const void* g_kv = p2 ? a_kv1 : a_kv0;
  const void* g_wq = p1 ? a_q1 : a_q0;
  const void* g_wk = p1 ? a_k1 : a_k0;
  const void* g_wv = p1 ? a_v1 : a_v0;
  const void* g_wo = p1 ? a_o1 : a_o0;
  const __amdgpu_buffer_rsrc_t ws = RSRC(a_ws, WS_END);
  TS(0);
  const unsigned xo = WS_XG + ph * 8192 + tid * 8;
  const unsigned f1own = WS_F1 + ((p1 * 4 + (lane & 3)) * 128 + src) * 4;
  unsigned xtag = 0u, xs[3] = {0u, 0u, 0u}, f1tag = 0u;
  v2u f1s = {0u, 0u};
  v4u xf, wq[8];
  if (wave < 4) {
    const int aw = wave;
    const __amdgpu_buffer_rsrc_t rx = RSRC(g_x, 4 * 512 * 2);
    const __amdgpu_buffer_rsrc_t rq = RSRC(g_wq, 512 * 4096 * 2);
    xtag = BLD32(ws, xo + cq * 2048 + 4, 0);
#pragma unroll
    for (int k = 0; k < 3; ++k) xs[k] = BLD32(ws, xo + 4, ((cq + 1 + k) & 3) * 2048);
    f1s = BLD64(ws, WS_F1 + pb * 512 + lane * 8, 0);
    f1tag = BLD32(ws, f1own, 0);
    SB();
#pragma unroll
    for (int jj = 0; jj < 8; ++jj) wq[jj] = BLD(rq, g * 8 * 8192 + h * 256 + r * 16, (128 * cq + 32 * aw + jj) * 8192, NT);
    xf = BLD(rx, (r >> 2) * 1024 + g * 16, (128 * cq + 32 * aw) * 2, 0);
    SB();
  }
  if (tid < C_N) ctl[tid] = 0u;
  asm volatile("s_waitcnt lgkmcnt(0)" ::: "memory");
  __builtin_amdgcn_s_barrier();

  if (wave < 4) {
    const int aw = wave;
    __builtin_amdgcn_s_setprio(XPRIO);
    TS(1);
    unsigned xval;
    {
      f32x4 acc[8];
#pragma unroll
      for (int i = 0; i < 8; ++i) acc[i] = mfma16(xf, tcol(wq, i), f32x4{0.f, 0.f, 0.f, 0.f});
      *(LAS f32x4*)(L + L_RED + (aw * 4 + g) * 512 + r * 32) = f32x4{acc[0][0], acc[1][0], acc[2][0], acc[3][0]};
      *(LAS f32x4*)(L + L_RED + (aw * 4 + g) * 512 + r * 32 + 16) = f32x4{acc[4][0], acc[5][0], acc[6][0], acc[7][0]};
      lds_signal(ctl + C_QP);
      lds_wait(ctl + C_QP, 4);
      f32x2 s = *(const LAS f32x2*)(L + L_RED + aw * 512 + lane * 8);
#pragma unroll
      for (int k = 1; k < 4; ++k) s += *(const LAS f32x2*)(L + L_RED + (k * 4 + aw) * 512 + lane * 8);
      xval = pk(s[0], s[1]);
    }
    xtag += 1u;
    BST64((v2u{xval, xtag}), ws, xo + cq * 2048);
    *(LAS unsigned*)(L + L_QP + (cq * 4 + aw) * 272 + lane * 4) = xval;
    TS(2);
#define HEARTBEAT(flags_too) do { xtag += 1u; BST64((v2u{xval, xtag}), ws, xo + cq * 2048); \
      if ((flags_too) && aw == 0) { f1tag += 1u; if (lane < 4) BST32(f1tag, ws, f1own); } } while (0)
    {
      v2u xv[3];
      __builtin_amdgcn_s_sleep(XS0);
      for (unsigned spins = 0; spins < SPIN_CAP; ++spins) {
        CFENCE();
#pragma unroll
        for (int k = 0; k < 3; ++k) xv[k] = BLD64(ws, xo, ((cq + 1 + k) & 3) * 2048);
        if (__all(xv[0].y != xs[0] && xv[1].y != xs[1] && xv[2].y != xs[2])) break;
        if ((spins & 3u) == 3u) HEARTBEAT(false);
        __builtin_amdgcn_s_sleep(XS);
      }
#pragma unroll
      for (int k = 0; k < 3; ++k) *(LAS unsigned*)(L + L_QP + (((cq + 1 + k) & 3) * 4 + aw) * 272 + lane * 4) = xv[k].x;
    }
    lds_signal(ctl + C_QB);
    TS(16);
    {
      lds_wait(ctl + C_WK, 1);
      v4u wk[2][4];
#pragma unroll
      for (int t = 0; t < 2; ++t)
#pragma unroll
        for (int s = 0; s < 4; ++s) wk[t][s] = *(const LAS v4u*)(L + L_WK + ((aw * 2 + t) * 4 + s) * 1024 + lane * 16);
      lds_wait(ctl + C_QB, 4);
      f32x4 a2[2] = {f32x4{0.f, 0.f, 0.f, 0.f}, f32x4{0.f, 0.f, 0.f, 0.f}};
#pragma unroll
      for (int s = 0; s < 4; ++s) {
        const v4u qf = *(const LAS v4u*)(L + L_QP + ((r & 3) * 4 + (r >> 2)) * 272 + s * 64 + g * 16);
        a2[0] = mfma16(qf, wk[0][s], a2[0]);
        a2[1] = mfma16(qf, wk[1][s], a2[1]);
      }
#pragma unroll
      for (int t = 0; t < 2; ++t) {
        const float v = ((a2[t][0] + a2[t][1]) + (a2[t][2] + a2[t][3])) * QSCALE;
        *(LAS unsigned short*)(L + L_QA + g * 256 + (32 * aw + 16 * t + r) * 2) = (unsigned short)(pk(v, v) & 0xFFFFu);
      }
    }
    lds_signal(ctl + C_QA);
    if (aw == 0) {
      lds_wait(ctl + C_QA, 5);
      const v4u v = *(const LAS v4u*)(L + L_QA + lane * 16);
      BST(v, ws, (cq == 3 && r >= 8) ? WS_Q7 + (((p1 * 4 + g) * 4 + ((r - 8) >> 1)) * 64 + (r & 1) * 32 + h) * 16
                                     : WS_QG + ((p1 * 4 + g) * 32 + h) * 1024 + cq * 256 + r * 16);
      lds_signal(ctl + C_QST);
      TS(14);
      wait_vm<0>();
      f1tag += 1u;
      if (lane < 4) BST32(f1tag, ws, f1own);
      TS(3);
    }
    const int r32 = lane & 31, hi = lane >> 5;
    v4u ta[32];
    const unsigned tbase = L_T + (32 * aw + r32) * 256;
    const unsigned tswz = ((r32 & 3) << 2) | ((r32 >> 2) & 3);
#define TA_LOAD(q) do { _Pragma("unroll") for (int s8 = 0; s8 < 8; ++s8) \
      ta[8 * (q) + s8] = *(const LAS v4u*)(L + tbase + (q) * 32768 + (((2 * s8 + hi) ^ tswz) << 4)); SB(); } while (0)
    lds_wait_slow(ctl + C_KV0, 1);
    TA_LOAD(0);
    lds_wait_slow(ctl + C_KV1, 1);
    TA_LOAD(1);
    if (aw == 0) {
      bool c0 = false, c1 = false;
      lds_wait_slow(ctl + C_PN, 1);
      for (unsigned spins = 0; spins < SPIN_CAP; ++spins) {
        CFENCE();
        const v2u f = BLD64(ws, WS_F1 + pb * 512 + lane * 8, 0);
        c0 = c0 || f.x != f1s.x; c1 = c1 || f.y != f1s.y;
        if (__all(c0 && c1)) break;
        if ((spins & 1u) == 1u) HEARTBEAT(true);
        __builtin_amdgcn_s_sleep(FS);
      }
      lds_signal(ctl + C_F1);
    } else {
      CFENCE();
      for (unsigned spins = 0; spins < LDS_CAP && lds_peek(ctl + C_F1) < 1u; ++spins) {
        __builtin_amdgcn_s_sleep(1);
        if ((spins & 127u) == 127u) HEARTBEAT(false);
      }
      CFENCE();
    }
    TS(4);
#define QRD(s) (*(const LAS v4u*)(L + L_SG + ((s) >> 2) * 4096 + r32 * 128 + (((2 * ((s) & 3) + hi) ^ ((r32 >> 1) & 7)) << 4)))
    v4u q7[4];
    {
      const int qrow = 8 * aw + (lane >> 3);
      v4u sg[7];
#pragma unroll
      for (int s = 0; s < 7; ++s) sg[s] = BLD(ws, qrow * 1024 + (lane & 7) * 16 + s * 128, WS_QG + pb * 32768, SC1);
#pragma unroll
      for (int s = 0; s < 4; ++s) q7[s] = BLD(ws, lane * 16 + s * 1024, WS_Q7 + pb * 4096, SC1);
      SB();
      lds_wait_slow(ctl + C_KV2, 1);
      TA_LOAD(2);
#pragma unroll
      for (int s = 0; s < 7; ++s) *(LAS v4u*)(L + L_SG + s * 4096 + qrow * 128 + (((lane & 7) ^ ((qrow >> 1) & 7)) << 4)) = sg[s];
      lds_signal(ctl + C_QS);
    }
    TS(5);
    lds_wait(ctl + C_KV3, 1);
    TA_LOAD(3);
    TS(6);
    lds_wait(ctl + C_QS, 4);
    TS(17);
    s16x4 tf[8][4][2];
    const int q4 = (lane & 15) >> 2, pp = lane & 3, blk = (lane >> 4) & 1;
#define TR_LOAD(ks, ct) do { _Pragma("unroll") for (int t = 0; t < 2; ++t) \
      tf[ks][ct][t] = __builtin_amdgcn_ds_read_tr16_b64_v4i16((LAS s16x4*)(L + L_T + aw * 32768 + (ks) * 4096 + (8 * t + 4 * hi + q4) * 256 + \
          (((ct) ^ q4) << 6) + (((2 * blk + (pp >> 1)) ^ ((2 * t + hi) & 3)) << 4) + ((pp & 1) << 3))); } while (0)
    f32x16 sa;
#pragma unroll
    for (int i = 0; i < 16; ++i) sa[i] = 0.f;
    {
      v4u qf[4];
#pragma unroll
      for (int s = 0; s < 4; ++s) qf[s] = QRD(s);
      SB();
#pragma unroll
      for (int s = 0; s < 32; ++s) {
        sa = mfma32(ta[s], s < 28 ? qf[s & 3] : q7[s & 3], sa);
        if (s + 4 < 28) qf[s & 3] = QRD(s + 4);
        if (s >= 1) TR_LOAD(((s - 1) >> 1) & 7, ((s - 1) >> 4) * 2 + ((s - 1) & 1));
        SB();
      }
      TR_LOAD(7, 3);
    }
    TC(22);
    float mx = sa[0];
#pragma unroll
    for (int i = 1; i < 16; ++i) mx = fmaxf(mx, sa[i]);
    mx = xor32_max(mx);
    *(LAS float*)(L + L_MX + r32 * 16 + aw * 4) = mx;
    lds_signal(ctl + C_MX);
    TC(23);
    lds_wait(ctl + C_MX, 4);
    TC(24);
    {
      const f32x4 m4 = *(const LAS f32x4*)(L + L_MX + r32 * 16);
      mx = fmaxf(fmaxf(m4[0], m4[1]), fmaxf(m4[2], m4[3]));
    }
    {
#pragma unroll
      for (int i = 0; i < 16; ++i) sa[i] = __builtin_amdgcn_exp2f(sa[i] - mx);
      *(LAS v4u*)(L + L_P + (2 * aw) * 1024 + lane * 16) = v4u{pk(sa[0], sa[1]), pk(sa[2], sa[3]), pk(sa[4], sa[5]), pk(sa[6], sa[7])};
      *(LAS v4u*)(L + L_P + (2 * aw + 1) * 1024 + lane * 16) = v4u{pk(sa[8], sa[9]), pk(sa[10], sa[11]), pk(sa[12], sa[13]), pk(sa[14], sa[15])};
      lds_signal(ctl + C_P);
      SB();
      float ps = (((sa[0] + sa[1]) + (sa[2] + sa[3])) + ((sa[4] + sa[5]) + (sa[6] + sa[7]))) +
                 (((sa[8] + sa[9]) + (sa[10] + sa[11])) + ((sa[12] + sa[13]) + (sa[14] + sa[15])));
      ps = xor32_sum(ps);
      *(LAS float*)(L + L_LS + r32 * 16 + aw * 4) = ps;
      lds_signal(ctl + C_LS);
    }
    TC(25);
    lds_wait(ctl + C_P, 4);
    TC(26);
    TS(7);
    v4u pf[8];
#pragma unroll
    for (int ks = 0; ks < 8; ++ks) pf[ks] = *(const LAS v4u*)(L + L_P + ks * 1024 + lane * 16);
    f32x16 oa[4];
#pragma unroll
    for (int ct = 0; ct < 4; ++ct)
#pragma unroll
      for (int i = 0; i < 16; ++i) oa[ct][i] = 0.f;
#define PV_MFMA(ks, ct) oa[ct] = mfma32(__builtin_bit_cast(v4u, __builtin_shufflevector(tf[ks][ct][0], tf[ks][ct][1], 0, 1, 2, 3, 4, 5, 6, 7)), pf[ks], oa[ct])
#define O_WRITE(half, ct, gg) *(LAS v2u*)(L + L_T + aw * 32768 + (half) * 4096 + r32 * 128 + (((4 * ((ct) & 1) + (gg)) ^ (r32 & 7)) << 4) + hi * 8) = \
      v2u{pk(oa[ct][4 * (gg)], oa[ct][4 * (gg) + 1]), pk(oa[ct][4 * (gg) + 2], oa[ct][4 * (gg) + 3])}
    const unsigned ord = L_T + aw * 32768 + (lane >> 3) * 128 + (((lane & 7) ^ ((lane >> 3) & 7)) << 4);
    const unsigned ost = WS_OG + (oblk + ((lane >> 3) * 4 + aw) * 128) * 256 + (lane & 7) * 16;
#pragma unroll
    for (int ks = 0; ks < 8; ++ks) { PV_MFMA(ks, 0); PV_MFMA(ks, 1); }
    SB();
    {
      v4u ov[4];
#pragma unroll
      for (int ks = 0; ks < 8; ++ks) {
        PV_MFMA(ks, 2);
        if (ks >= 1 && ks <= 4) { O_WRITE(0, 0, ks - 1); }
        SB();
        PV_MFMA(ks, 3);
        if (ks >= 1 && ks <= 4) { O_WRITE(0, 1, ks - 1); }
        if (ks == 5) {
          CFENCE();
#pragma unroll
          for (int u = 0; u < 4; ++u) ov[u] = *(const LAS v4u*)(L + ord + u * 1024);
        }
        if (ks == 7) {
#pragma unroll
          for (int u = 0; u < 4; ++u) __builtin_amdgcn_raw_buffer_store_b128(ov[u], ws, (int)ost, u * 32 * 32768, SC1);
        }
        SB();
      }
    }
    TC(27);
    TS(8);
#pragma unroll
    for (int gg = 0; gg < 4; ++gg) { O_WRITE(1, 2, gg); O_WRITE(1, 3, gg); }
    CFENCE();
    {
      v4u ov[4];
#pragma unroll
      for (int u = 0; u < 4; ++u) ov[u] = *(const LAS v4u*)(L + ord + 4096 + u * 1024);
#pragma unroll
      for (int u = 0; u < 4; ++u) __builtin_amdgcn_raw_buffer_store_b128(ov[u], ws, (int)(ost + 128), u * 32 * 32768, SC1);
    }
    TS(15);
    TC(29);
    {
      lds_wait(ctl + C_LS, 4);
      const f32x4 l4 = *(const LAS f32x4*)(L + L_LS + r32 * 16);
      const float lsum = (l4[0] + l4[1]) + (l4[2] + l4[3]);
      wait_vm<0>();
      if (hi == 0) BST64((v2u{__float_as_uint(mx), __float_as_uint(lsum) | 1u}), ws, WS_F2 + (oblk + (r32 * 4 + aw) * 128) * 8);
    }
    TC(30);
    TS(9);
    {
      v4u st[4];
      __builtin_amdgcn_s_sleep(DS0);
      const unsigned fo = WS_F2 + blk1 * 1024 + aw * 256 + g * 64;
      for (unsigned spins = 0; spins < SPIN_CAP; ++spins) {
        CFENCE();
#pragma unroll
        for (int k = 0; k < 4; ++k) st[k] = BLD(ws, fo + k * 16, 0, SC1);
        bool ok = true;
#pragma unroll
        for (int k = 0; k < 4; ++k) ok = ok && (st[k].y != 0u) && (st[k].w != 0u);
        if (__all(ok)) break;
        if ((spins & 7u) == 7u) HEARTBEAT(true);
        __builtin_amdgcn_s_sleep(DS);
      }
      CFENCE();
      TS(10);
      v4u od[8];
#pragma unroll
      for (int i = 0; i < 8; ++i) od[i] = BLD(ws, (8 * g + i) * 256 + r * 16, WS_OG + blk1 * 32768 + aw * 8192, SC1);
      SB();
      lds_signal(ctl + C_OL);
      float m[8], l[8];
#pragma unroll
      for (int k = 0; k < 4; ++k) {
        m[2 * k] = __uint_as_float(st[k].x); l[2 * k] = __uint_as_float(st[k].y);
        m[2 * k + 1] = __uint_as_float(st[k].z); l[2 * k + 1] = __uint_as_float(st[k].w);
      }
      float M = m[0];
#pragma unroll
      for (int i = 1; i < 8; ++i) M = fmaxf(M, m[i]);
      M = xor32_max(xor16_max(M));
      float Ls = 0.f;
#pragma unroll
      for (int i = 0; i < 8; ++i) { m[i] = __builtin_amdgcn_exp2f(m[i] - M); Ls += m[i] * l[i]; }
      Ls = xor32_sum(xor16_sum(Ls));
      const float inv = 1.0f / Ls;
      v4u wf;
#pragma unroll
      for (int k = 0; k < 4; ++k) {
        const float w0 = m[2 * k] * inv, w1 = m[2 * k + 1] * inv;
        const unsigned hi2 = pk(w0, w1);
        const unsigned lo2 = pk(w0 - __uint_as_float(hi2 << 16), w1 - __uint_as_float(hi2 & 0xFFFF0000u));
        wf[k] = (r & 1) ? lo2 : hi2;
      }
      SB();
      f32x4 la[8];
#pragma unroll
      for (int e = 0; e < 8; ++e) la[e] = mfma16(wf, tcol(od, e), f32x4{0.f, 0.f, 0.f, 0.f});
      const v4u o = {pk(la[0][0] + la[0][1], la[1][0] + la[1][1]), pk(la[2][0] + la[2][1], la[3][0] + la[3][1]),
                     pk(la[4][0] + la[4][1], la[5][0] + la[5][1]), pk(la[6][0] + la[6][1], la[7][0] + la[7][1])};
      if (g == 0) *(LAS v4u*)(L + L_QA + aw * 256 + r * 16) = o;
      lds_signal(ctl + C_LAT);
      TS(11);
    }
    if (aw == 0) {
      const int eb = h & 3, em = 4 * ((h >> 2) & 1) + (h >> 3);
      lds_wait_slow(ctl + C_WST, 4);
      v4u pv;
      const unsigned po = WS_X1 + (p1 * 128 + (lane >> 1) * 4 + cq) * 2048 + 1024 + eb * 256 + em * 32 + (lane & 1) * 16;
      for (unsigned spins = 0; spins < SPIN_CAP; ++spins) {
        CFENCE();
        pv = BLD(ws, po, 0, SC1);
        if (__all(pv.x != SENT && pv.y != SENT && pv.z != SENT && pv.w != SENT)) break;
        __builtin_amdgcn_s_sleep(1);
      }
      TS(12);
      float s[8];
#pragma unroll
      for (int c = 0; c < 4; ++c) { s[2 * c] = __uint_as_float(pv[c] << 16); s[2 * c + 1] = __uint_as_float(pv[c] & 0xFFFF0000u); }
#pragma unroll
      for (int c = 0; c < 8; ++c) {
        float v = s[c];
        v = dpp_add<0x4E>(v);
        v = dpp_add<0x124>(v);
        v = dpp_add<0x128>(v);
        v = xor32_sum(xor16_sum(v));
        s[c] = v;
      }
      if (lane < 2) {
        const v4u o = {pk(s[0], s[1]), pk(s[2], s[3]), pk(s[4], s[5]), pk(s[6], s[7])};
        *(GAS v4u*)((char*)a_out + (eb * 1024 + p1 * 512 + cq * 128 + em * 16 + lane * 8) * 2) = o;
      }
      TS(13);
    }
  } else {
    const int lw = wave - 4;
    if (lw == 0) {
      const unsigned lds0 = (unsigned)(size_t)L;
      const unsigned long long tk0 = __builtin_amdgcn_s_memtime();
      __builtin_amdgcn_s_setprio(3);
      {
        const GAS char* a = (const GAS char*)g_wk + (size_t)(128 * cq) * 8192 + h * 256;
        if (lane == 1) a = (const GAS char*)g_kv + ((size_t)b2 * 4096 + 128 * j) * 1024;
        if (lane == 2) a = (const GAS char*)g_wv + (size_t)(128 * cq) * 8192 + h * 256;
        if (lane == 3) a = (const GAS char*)g_wo + (size_t)(h * 128) * 1024 + cq * 256;
        if (lane == 4) a = (const GAS char*)a_ws + WS_OG + p2 * 4194304 + 4096;
        if (lane == 5) a = (const GAS char*)a_ws + WS_OG + p2 * 4194304 + 2097152 + 4096;
        if (lane == 6) a = (const GAS char*)a_ws + WS_OG + blk1 * 32768;
        if (lane < 7) { const unsigned t = __builtin_nontemporal_load((const GAS unsigned*)a); asm volatile("" :: "v"(t)); }
      }
      lds_wait(ctl + C_QP, 1);
      {
        const char* wkb = (const char*)g_wk + (size_t)(128 * cq) * 8192 + h * 256;
        const unsigned vk = r * 8192 + g * 16;
        for (int G = 0; G < 4; ++G) {
#pragma unroll
          for (int i = 0; i < 8; ++i) dma16(wkb + (size_t)(G * 2 + (i >> 2)) * 16 * 8192 + (i & 3) * 64, vk, lds0 + L_WK + (G * 8 + i) * 1024);
        }
        wait_vm<0>();
        lds_signal(ctl + C_WK);
        while (lds_peek(ctl + C_QST) < 1u && __builtin_amdgcn_s_memtime() - tk0 < (unsigned long long)TRES) __builtin_amdgcn_s_sleep(1);
        CFENCE();
      }
      const char* kvb = (const char*)g_kv + ((size_t)b2 * 4096 + 128 * j) * 1024;
      unsigned vo[4];
#pragma unroll
      for (int u3 = 0; u3 < 4; ++u3) vo[u3] = g * 1024 + ((r ^ ((g << 2) | u3)) << 4);
      for (int G = 0; G < 16; ++G) {
        wait_vm<KDEPTH - 8>();
        const int done = 8 * G - (KDEPTH - 8);
        if (G == 12) lds_wait_slow(ctl + C_QST, 1);
        if (G == PG) lds_signal(ctl + C_PN);
        if (done == 32) lds_signal(ctl + C_KV0);
        if (done == 64) lds_signal(ctl + C_KV1);
        if (done == 96) lds_signal(ctl + C_KV2);
        const int q = G >> 2, u0 = (G & 3) * 8;
        const char* gb = kvb + u0 * 4096 + q * 256;
        const unsigned lb = lds0 + L_T + q * 32768 + u0 * 1024;
#pragma unroll
        for (int i = 0; i < 8; ++i) dma16(gb + i * 4096, vo[i & 3], lb + i * 1024);
      }
      TS(1);
      if (120 - (KDEPTH - 8) < 96) { wait_vm<32>(); lds_signal(ctl + C_KV2); }
      wait_vm<0>();
      lds_signal(ctl + C_KV3);
      __builtin_amdgcn_s_setprio(0);
      TS(2);
    } else if (lw == 3) {
      {
        const v4u z = {0u, 0u, 0u, 0u}, sn = {SENT, SENT, SENT, SENT};
        BST(z, ws, WS_F2 + blk1 * 1024 + lane * 16);
        BST(sn, ws, WS_X1 + blk1 * 2048 + lane * 16);
        BST(sn, ws, WS_X1 + blk1 * 2048 + 1024 + lane * 16);
        wait_vm<0>();
        lds_signal(ctl + C_QA);
      }
    }
    const __amdgpu_buffer_rsrc_t rv = RSRC(g_wv, 512 * 4096 * 2);
    const __amdgpu_buffer_rsrc_t ro = RSRC(g_wo, 512 * 4096 * 2);
    v4u wv[8], wo[8];
    if (WISS) lds_wait_slow(ctl + C_KV3, 1);
    lds_wait_slow(ctl + C_QS, 4);
#pragma unroll
    for (int jj = 0; jj < 8; ++jj) wv[jj] = BLD(rv, g * 8 * 8192 + h * 256 + r * 16, (128 * cq + 32 * lw + jj) * 8192, NT);
    SB();
    TS(3);
    v4u tv[8], to[8];
#pragma unroll
    for (int i = 0; i < 8; ++i) { tv[i] = tcol(wv, i); asm volatile("" : "+v"(tv[i])); }
    lds_wait_slow(ctl + C_OL, 4);
#pragma unroll
    for (int jj = 0; jj < 8; ++jj) wo[jj] = BLD(ro, g * 8 * 1024 + r * 16, (h * 128 + 32 * lw + jj) * 1024 + cq * 256, NT);
    SB();
    TS(4);
    lds_wait_slow(ctl + C_LAT, 4);
    {
      const v4u lf = *(const LAS v4u*)(L + L_QA + (r >> 2) * 256 + lw * 64 + g * 16);
      f32x4 ca[8];
#pragma unroll
      for (int i = 0; i < 8; ++i) ca[i] = mfma16(lf, tv[i], f32x4{0.f, 0.f, 0.f, 0.f});
      *(LAS f32x4*)(L + L_RED + (lw * 4 + g) * 512 + r * 32) = f32x4{ca[0][0], ca[1][0], ca[2][0], ca[3][0]};
      *(LAS f32x4*)(L + L_RED + (lw * 4 + g) * 512 + r * 32 + 16) = f32x4{ca[4][0], ca[5][0], ca[6][0], ca[7][0]};
      lds_signal(ctl + C_CTX);
      lds_wait(ctl + C_CTX, 4);
      f32x2 s = *(const LAS f32x2*)(L + L_RED + lw * 512 + lane * 8);
#pragma unroll
      for (int k = 1; k < 4; ++k) s += *(const LAS f32x2*)(L + L_RED + (k * 4 + lw) * 512 + lane * 8);
      const unsigned cv = pk_ns(s[0], s[1]);
      BST32(cv, ws, WS_X1 + blk1 * 2048 + lw * 256 + lane * 4);
      *(LAS unsigned*)(L + L_QP + (cq * 4 + lw) * 272 + lane * 4) = cv;
    }
    TS(5);
#pragma unroll
    for (int i = 0; i < 8; ++i) { to[i] = tcol(wo, i); asm volatile("" : "+v"(to[i])); }
    {
      unsigned xv[3];
      const unsigned xb = WS_X1 + (p1 * 128 + h * 4) * 2048 + lw * 256 + lane * 4;
      for (unsigned spins = 0; spins < SPIN_CAP; ++spins) {
        CFENCE();
#pragma unroll
        for (int k = 0; k < 3; ++k) xv[k] = BLD32(ws, xb, ((cq + 1 + k) & 3) * 2048);
        if (__all(xv[0] != SENT && xv[1] != SENT && xv[2] != SENT)) break;
        __builtin_amdgcn_s_sleep(1);
      }
#pragma unroll
      for (int k = 0; k < 3; ++k) *(LAS unsigned*)(L + L_QP + (((cq + 1 + k) & 3) * 4 + lw) * 272 + lane * 4) = xv[k];
    }
    lds_signal(ctl + C_X1);
    TS(7);
    lds_wait(ctl + C_X1, 4);
    {
      const v4u cf = *(const LAS v4u*)(L + L_QP + ((r & 3) * 4 + (r >> 2)) * 272 + lw * 64 + g * 16);
      float o[8];
#pragma unroll
      for (int i = 0; i < 8; ++i) {
        const f32x4 a = mfma16(cf, to[i], f32x4{0.f, 0.f, 0.f, 0.f});
        o[i] = (a[0] + a[1]) + (a[2] + a[3]);
      }
      *(LAS f32x4*)(L + L_RED3 + (lw * 4 + g) * 512 + r * 32) = f32x4{o[0], o[1], o[2], o[3]};
      *(LAS f32x4*)(L + L_RED3 + (lw * 4 + g) * 512 + r * 32 + 16) = f32x4{o[4], o[5], o[6], o[7]};
      lds_signal(ctl + C_O3);
      lds_wait(ctl + C_O3, 4);
      f32x2 s = *(const LAS f32x2*)(L + L_RED3 + lw * 512 + lane * 8);
#pragma unroll
      for (int k = 1; k < 4; ++k) s += *(const LAS f32x2*)(L + L_RED3 + (k * 4 + lw) * 512 + lane * 8);
      BST32(pk_ns(s[0], s[1]), ws, WS_X1 + blk1 * 2048 + 1024 + lw * 256 + lane * 4);
    }
    lds_signal(ctl + C_WST);
    TS(6);
  }
}

extern "C" void kernel_launch(void* const* d_in, const int* in_sizes, int n_in, void* d_out, int out_size, void* d_ws,
                              size_t ws_size, hipStream_t stream) {
  Params P;
  for (int p = 0; p < 2; ++p) {
    P.x[p] = d_in[6 * p + 0]; P.ckv[p] = d_in[6 * p + 1]; P.wq[p] = d_in[6 * p + 2];
    P.wuk[p] = d_in[6 * p + 3]; P.wuv[p] = d_in[6 * p + 4]; P.wo[p] = d_in[6 * p + 5];
  }
  P.out = d_out; P.ws = d_ws;
  (void)hipFuncSetAttribute((const void*)mla_fused, hipFuncAttributeMaxDynamicSharedMemorySize, (int)L_END);
  mla_fused<<<256, 512, L_END, stream>>>(P);
}
```

```cpp
#include <hip/hip_runtime.h>
#include <hip/hip_bf16.h>

#define GAS __attribute__((address_space(1)))
#define LAS __attribute__((address_space(3)))
typedef unsigned v4u __attribute__((ext_vector_type(4)));
typedef unsigned v2u __attribute__((ext_vector_type(2)));
typedef float f32x2 __attribute__((ext_vector_type(2)));
typedef float f32x4 __attribute__((ext_vector_type(4)));
typedef float f32x16 __attribute__((ext_vector_type(16)));
typedef short bf16x8 __attribute__((ext_vector_type(8)));
typedef short s16x4 __attribute__((ext_vector_type(4)));
#define RLX_WG __ATOMIC_RELAXED, __HIP_MEMORY_SCOPE_WORKGROUP
#define DI __device__ __forceinline__

constexpr int KDEPTH = 40;
constexpr int PG = 12;
constexpr int TRES = 7000;
constexpr int CODE_TAIL = 20;
#define XS0 24
#define XS 8
#define FS 16
#define DS0 16
#define DS 3

constexpr unsigned WS_F1 = 0;
constexpr unsigned WS_XG = 4096;
constexpr unsigned WS_QG = WS_XG + 64 * 4 * 2048;
constexpr unsigned WS_Q7 = WS_QG + 8 * 32768;
constexpr unsigned WS_F2 = WS_Q7 + 8 * 4096;
constexpr unsigned WS_X1 = WS_F2 + 256 * 1024;
constexpr unsigned WS_OG = WS_X1 + 256 * 2048;
constexpr unsigned WS_END = WS_OG + 256 * 32768;

constexpr unsigned L_T = 0;
constexpr unsigned L_WK = 3 * 32768;
constexpr unsigned L_CT = 131072;
constexpr unsigned L_MX = L_CT + 128;
constexpr unsigned L_QA = L_MX + 512;
constexpr unsigned L_SG = L_QA + 1024;
constexpr unsigned L_RED = L_SG;
constexpr unsigned L_QP = L_SG + 8192;
constexpr unsigned L_P = L_SG + 16384;
constexpr unsigned L_RED3 = L_SG + 16384;
constexpr unsigned L_LS = L_SG + 28 * 1024;
constexpr unsigned L_END = L_LS + 512;
enum { C_QP = 0, C_QB, C_QA, C_F1, C_QS, C_KV0, C_KV1, C_KV2, C_KV3, C_MX, C_P, C_LS,
       C_LAT, C_CTX, C_WST, C_WK, C_QST, C_X1, C_O3, C_PN, C_OL, C_N };

constexpr unsigned SENT = 0xFFFFFFFFu;
constexpr unsigned SPIN_CAP = 1u << 16;
constexpr unsigned LDS_CAP = 1u << 22;
constexpr float QSCALE = 0.08838834764831845f * 1.4426950408889634f;

struct Params {
  const void* x[2]; const void* ckv[2]; const void* wq[2]; const void* wuk[2]; const void* wuv[2]; const void* wo[2];
  void* out; void* ws;
};

DI f32x4 mfma16(v4u a, v4u b, f32x4 c) {
  return __builtin_amdgcn_mfma_f32_16x16x32_bf16(__builtin_bit_cast(bf16x8, a), __builtin_bit_cast(bf16x8, b), c, 0, 0, 0);
}
DI f32x16 mfma32(v4u a, v4u b, f32x16 c) {
  return __builtin_amdgcn_mfma_f32_32x32x16_bf16(__builtin_bit_cast(bf16x8, a), __builtin_bit_cast(bf16x8, b), c, 0, 0, 0);
}
typedef __bf16 bf2_t __attribute__((ext_vector_type(2)));
DI unsigned pk(float lo, float hi) {
  f32x2 f = {lo, hi};
  return __builtin_bit_cast(unsigned, __builtin_convertvector(f, bf2_t));
}
DI unsigned pk_ns(float lo, float hi) {
  const unsigned u = pk(lo, hi);
  return (u == SENT) ? 0x7FC07FC0u : u;
}
DI v4u tcol(const v4u (&w)[8], int i) {
  const unsigned sel = (i & 1) ? 0x07060302u : 0x05040100u;
  v4u o;
  o.x = __builtin_amdgcn_perm(w[1][i >> 1], w[0][i >> 1], sel);
  o.y = __builtin_amdgcn_perm(w[3][i >> 1], w[2][i >> 1], sel);
  o.z = __builtin_amdgcn_perm(w[5][i >> 1], w[4][i >> 1], sel);
  o.w = __builtin_amdgcn_perm(w[7][i >> 1], w[6][i >> 1], sel);
  return o;
}
#define CFENCE() asm volatile("" ::: "memory")
#define SB() __builtin_amdgcn_sched_barrier(0)
DI void lds_signal(LAS unsigned* c) {
  unsigned long long keep;
  asm volatile("s_mov_b64 %0, exec\n\ts_mov_b64 exec, 1\n\tds_add_u32 %1, %2\n\ts_mov_b64 exec, %0"
               : "=&s"(keep) : "v"((unsigned)(size_t)c), "v"(1u) : "memory");
}
DI unsigned lds_peek(LAS unsigned* c) { return (unsigned)__builtin_amdgcn_readfirstlane(__hip_atomic_load(c, RLX_WG)); }
DI void lds_wait_slow(LAS unsigned* c, unsigned target) {
  CFENCE();
  for (unsigned n = 0; n < LDS_CAP && lds_peek(c) < target; ++n) __builtin_amdgcn_s_sleep(1);
  CFENCE();
}
DI void lds_wait(LAS unsigned* c, unsigned target) {
  CFENCE();
  for (unsigned n = 0; n < LDS_CAP && lds_peek(c) < target; ++n) {}
  CFENCE();
}
DI float xor32_max(float v) {
  auto r = __builtin_amdgcn_permlane32_swap(__float_as_uint(v), __float_as_uint(v), false, false);
  return fmaxf(__uint_as_float(r[0]), __uint_as_float(r[1]));
}
DI float xor32_sum(float v) {
  auto r = __builtin_amdgcn_permlane32_swap(__float_as_uint(v), __float_as_uint(v), false, false);
  return __uint_as_float(r[0]) + __uint_as_float(r[1]);
}
DI float xor16_max(float v) {
  auto r = __builtin_amdgcn_permlane16_swap(__float_as_uint(v), __float_as_uint(v), false, false);
  return fmaxf(__uint_as_float(r[0]), __uint_as_float(r[1]));
}
DI float xor16_sum(float v) {
  auto r = __builtin_amdgcn_permlane16_swap(__float_as_uint(v), __float_as_uint(v), false, false);
  return __uint_as_float(r[0]) + __uint_as_float(r[1]);
}
template <int CTRL> DI float dpp_add(float v) {
  return v + __uint_as_float(__builtin_amdgcn_update_dpp(0u, __float_as_uint(v), CTRL, 0xF, 0xF, false));
}
DI void dma16(const void* sbase, unsigned voff, unsigned lds_dst) {
  unsigned keep;
  asm volatile("s_mov_b32 %0, m0\n\ts_mov_b32 m0, %3\n\ts_nop 0\n\tglobal_load_lds_dwordx4 %1, %2 nt\n\ts_mov_b32 m0, %0"
               : "=&s"(keep) : "v"(voff), "s"(sbase), "s"(lds_dst) : "memory");
}
template <int N> DI void wait_vm() { asm volatile("s_waitcnt vmcnt(%0)" ::"i"(N) : "memory"); }
#define RSRC(ptr, bytes) __builtin_amdgcn_make_buffer_rsrc((void*)(ptr), 0, (int)(bytes), 0x00020000)
#define BLD(rs, vo, so, aux) __builtin_amdgcn_raw_buffer_load_b128(rs, (int)(vo), (int)(so), aux)
#define BLD64(rs, vo, so) __builtin_amdgcn_raw_buffer_load_b64(rs, (int)(vo), (int)(so), SC1)
#define BLD32(rs, vo, so) __builtin_amdgcn_raw_buffer_load_b32(rs, (int)(vo), (int)(so), SC1)
#define BST(v, rs, vo) __builtin_amdgcn_raw_buffer_store_b128(v, rs, (int)(vo), 0, SC1)
#define BST64(v, rs, vo) __builtin_amdgcn_raw_buffer_store_b64(v, rs, (int)(vo), 0, SC1)
#define BST32(v, rs, vo) __builtin_amdgcn_raw_buffer_store_b32(v, rs, (int)(vo), 0, SC1)
constexpr int SC1 = 16, NT = 2;

__global__ void __launch_bounds__(512, 2) mla_fused(Params P) {
  extern __shared__ __attribute__((aligned(16))) unsigned char smem[];
  LAS unsigned char* L = (LAS unsigned char*)smem;
  LAS unsigned* ctl = (LAS unsigned*)(L + L_CT);
  const int tid = threadIdx.x, lane = tid & 63;
  const int wave = __builtin_amdgcn_readfirstlane(tid >> 6);
  const int w = blockIdx.x;
  const int ph = w & 63, p1 = ph >> 5, h = w & 31, cq = w >> 6;
  const int src = h * 4 + cq;
  const int blk1 = p1 * 128 + src;
  const int pb = w & 7, p2 = pb >> 2, b2 = pb & 3, j = w >> 3;
  const unsigned oblk = p2 * 128 * 128 + b2 * 32 + j;
  const int r = lane & 15, g = lane >> 4;
  unsigned c0 = 0u, c1 = 0u, c2 = 0u;
  if (wave == 5 && j < 4) {
    unsigned long long pc;
    asm volatile("s_getpc_b64 %0" : "=s"(pc));
    const GAS char* cb = (const GAS char*)(pc & ~0xFFull) + lane * 128;
    c0 = *(const GAS unsigned*)(cb);
    c1 = *(const GAS unsigned*)(cb + 8192);
    c2 = *(const GAS unsigned*)(cb + (lane < CODE_TAIL ? 16384 : 0));
  }
  const void *a_x0 = P.x[0], *a_x1 = P.x[1], *a_kv0 = P.ckv[0], *a_kv1 = P.ckv[1], *a_q0 = P.wq[0], *a_q1 = P.wq[1], *a_k0 = P.wuk[0],
             *a_k1 = P.wuk[1], *a_v0 = P.wuv[0], *a_v1 = P.wuv[1], *a_o0 = P.wo[0], *a_o1 = P.wo[1];
  void *a_out = P.out, *a_ws = P.ws;
  asm volatile("" : "+s"(a_x0), "+s"(a_x1), "+s"(a_kv0), "+s"(a_kv1), "+s"(a_q0), "+s"(a_q1), "+s"(a_k0), "+s"(a_k1), "+s"(a_v0), "+s"(a_v1),
                    "+s"(a_o0), "+s"(a_o1), "+s"(a_out), "+s"(a_ws));
  const void* g_x = p1 ? a_x1 : a_x0;
  const void* g_kv = p2 ? a_kv1 : a_kv0;
  const void* g_wq = p1 ? a_q1 : a_q0;
  const void* g_wk = p1 ? a_k1 : a_k0;
  const void* g_wv = p1 ? a_v1 : a_v0;
  const void* g_wo = p1 ? a_o1 : a_o0;
  const __amdgpu_buffer_rsrc_t ws = RSRC(a_ws, WS_END);
  const unsigned xo = WS_XG + ph * 8192 + tid * 8;
  const unsigned f1own = WS_F1 + ((p1 * 4 + (lane & 3)) * 128 + src) * 4;
  unsigned xtag = 0u, xs[3] = {0u, 0u, 0u}, f1tag = 0u;
  v2u f1s = {0u, 0u};
  v4u xf, wq[8];
  if (wave < 4) {
    const int aw = wave;
    const __amdgpu_buffer_rsrc_t rx = RSRC(g_x, 4 * 512 * 2);
    const __amdgpu_buffer_rsrc_t rq = RSRC(g_wq, 512 * 4096 * 2);
    xtag = BLD32(ws, xo + cq * 2048 + 4, 0);
#pragma unroll
    for (int k = 0; k < 3; ++k) xs[k] = BLD32(ws, xo + 4, ((cq + 1 + k) & 3) * 2048);
    f1s = BLD64(ws, WS_F1 + pb * 512 + lane * 8, 0);
    f1tag = BLD32(ws, f1own, 0);
    SB();
#pragma unroll
    for (int jj = 0; jj < 8; ++jj) wq[jj] = BLD(rq, g * 8 * 8192 + h * 256 + r * 16, (128 * cq + 32 * aw + jj) * 8192, NT);
    xf = BLD(rx, (r >> 2) * 1024 + g * 16, (128 * cq + 32 * aw) * 2, 0);
    SB();
  }
  if (tid < C_N) ctl[tid] = 0u;
  asm volatile("s_waitcnt lgkmcnt(0)" ::: "memory");
  __builtin_amdgcn_s_barrier();

  if (wave < 4) {
    const int aw = wave;
    __builtin_amdgcn_s_setprio(1);
    unsigned xval;
    {
      f32x4 acc[8];
#pragma unroll
      for (int i = 0; i < 8; ++i) acc[i] = mfma16(xf, tcol(wq, i), f32x4{0.f, 0.f, 0.f, 0.f});
      *(LAS f32x4*)(L + L_RED + (aw * 4 + g) * 512 + r * 32) = f32x4{acc[0][0], acc[1][0], acc[2][0], acc[3][0]};
      *(LAS f32x4*)(L + L_RED + (aw * 4 + g) * 512 + r * 32 + 16) = f32x4{acc[4][0], acc[5][0], acc[6][0], acc[7][0]};
      lds_signal(ctl + C_QP);
      lds_wait(ctl + C_QP, 4);
      f32x2 s = *(const LAS f32x2*)(L + L_RED + aw * 512 + lane * 8);
#pragma unroll
      for (int k = 1; k < 4; ++k) s += *(const LAS f32x2*)(L + L_RED + (k * 4 + aw) * 512 + lane * 8);
      xval = pk(s[0], s[1]);
    }
    xtag += 1u;
    BST64((v2u{xval, xtag}), ws, xo + cq * 2048);
    *(LAS unsigned*)(L + L_QP + (cq * 4 + aw) * 272 + lane * 4) = xval;
#define HEARTBEAT(flags_too) do { xtag += 1u; BST64((v2u{xval, xtag}), ws, xo + cq * 2048); \
      if ((flags_too) && aw == 0) { f1tag += 1u; if (lane < 4) BST32(f1tag, ws, f1own); } } while (0)
    {
      v2u xv[3];
      __builtin_amdgcn_s_sleep(XS0);
      for (unsigned spins = 0; spins < SPIN_CAP; ++spins) {
        CFENCE();
#pragma unroll
        for (int k = 0; k < 3; ++k) xv[k] = BLD64(ws, xo, ((cq + 1 + k) & 3) * 2048);
        if (__all(xv[0].y != xs[0] && xv[1].y != xs[1] && xv[2].y != xs[2])) break;
        if ((spins & 3u) == 3u) HEARTBEAT(false);
        __builtin_amdgcn_s_sleep(XS);
      }
#pragma unroll
      for (int k = 0; k < 3; ++k) *(LAS unsigned*)(L + L_QP + (((cq + 1 + k) & 3) * 4 + aw) * 272 + lane * 4) = xv[k].x;
    }
    lds_signal(ctl + C_QB);
    {
      lds_wait(ctl + C_WK, 1);
      v4u wk[2][4];
#pragma unroll
      for (int t = 0; t < 2; ++t)
#pragma unroll
        for (int s = 0; s < 4; ++s) wk[t][s] = *(const LAS v4u*)(L + L_WK + ((aw * 2 + t) * 4 + s) * 1024 + lane * 16);
      lds_wait(ctl + C_QB, 4);
      f32x4 a2[2] = {f32x4{0.f, 0.f, 0.f, 0.f}, f32x4{0.f, 0.f, 0.f, 0.f}};
#pragma unroll
      for (int s = 0; s < 4; ++s) {
        const v4u qf = *(const LAS v4u*)(L + L_QP + ((r & 3) * 4 + (r >> 2)) * 272 + s * 64 + g * 16);
        a2[0] = mfma16(qf, wk[0][s], a2[0]);
        a2[1] = mfma16(qf, wk[1][s], a2[1]);
      }
#pragma unroll
      for (int t = 0; t < 2; ++t) {
        const float v = ((a2[t][0] + a2[t][1]) + (a2[t][2] + a2[t][3])) * QSCALE;
        *(LAS unsigned short*)(L + L_QA + g * 256 + (32 * aw + 16 * t + r) * 2) = (unsigned short)(pk(v, v) & 0xFFFFu);
      }
    }
    lds_signal(ctl + C_QA);
    if (aw == 0) {
      lds_wait(ctl + C_QA, 5);
      const v4u v = *(const LAS v4u*)(L + L_QA + lane * 16);
      BST(v, ws, (cq == 3 && r >= 8) ? WS_Q7 + (((p1 * 4 + g) * 4 + ((r - 8) >> 1)) * 64 + (r & 1) * 32 + h) * 16
                                     : WS_QG + ((p1 * 4 + g) * 32 + h) * 1024 + cq * 256 + r * 16);
      lds_signal(ctl + C_QST);
      wait_vm<0>();
      f1tag += 1u;
      if (lane < 4) BST32(f1tag, ws, f1own);
    }
    const int r32 = lane & 31, hi = lane >> 5;
    v4u ta[32];
    const unsigned tbase = L_T + (32 * aw + r32) * 256;
    const unsigned tswz = ((r32 & 3) << 2) | ((r32 >> 2) & 3);
#define TA_LOAD(q) do { _Pragma("unroll") for (int s8 = 0; s8 < 8; ++s8) \
      ta[8 * (q) + s8] = *(const LAS v4u*)(L + tbase + (q) * 32768 + (((2 * s8 + hi) ^ tswz) << 4)); SB(); } while (0)
    lds_wait_slow(ctl + C_KV0, 1);
    TA_LOAD(0);
    lds_wait_slow(ctl + C_KV1, 1);
    TA_LOAD(1);
    if (aw == 0) {
      bool c0 = false, c1 = false;
      lds_wait_slow(ctl + C_PN, 1);
      for (unsigned spins = 0; spins < SPIN_CAP; ++spins) {
        CFENCE();
        const v2u f = BLD64(ws, WS_F1 + pb * 512 + lane * 8, 0);
        c0 = c0 || f.x != f1s.x; c1 = c1 || f.y != f1s.y;
        if (__all(c0 && c1)) break;
        if ((spins & 1u) == 1u) HEARTBEAT(true);
        __builtin_amdgcn_s_sleep(FS);
      }
      lds_signal(ctl + C_F1);
    } else {
      CFENCE();
      for (unsigned spins = 0; spins < LDS_CAP && lds_peek(ctl + C_F1) < 1u; ++spins) {
        __builtin_amdgcn_s_sleep(1);
        if ((spins & 127u) == 127u) HEARTBEAT(false);
      }
      CFENCE();
    }
#define QRD(s) (*(const LAS v4u*)(L + L_SG + ((s) >> 2) * 4096 + r32 * 128 + (((2 * ((s) & 3) + hi) ^ ((r32 >> 1) & 7)) << 4)))
    v4u q7[4];
    {
      const int qrow = 8 * aw + (lane >> 3);
      v4u sg[7];
#pragma unroll
      for (int s = 0; s < 7; ++s) sg[s] = BLD(ws, qrow * 1024 + (lane & 7) * 16 + s * 128, WS_QG + pb * 32768, SC1);
#pragma unroll
      for (int s = 0; s < 4; ++s) q7[s] = BLD(ws, lane * 16 + s * 1024, WS_Q7 + pb * 4096, SC1);
      SB();
      lds_wait_slow(ctl + C_KV2, 1);
      TA_LOAD(2);
#pragma unroll
      for (int s = 0; s < 7; ++s) *(LAS v4u*)(L + L_SG + s * 4096 + qrow * 128 + (((lane & 7) ^ ((qrow >> 1) & 7)) << 4)) = sg[s];
      lds_signal(ctl + C_QS);
    }
    lds_wait(ctl + C_KV3, 1);
    TA_LOAD(3);
    lds_wait(ctl + C_QS, 4);
    s16x4 tf[8][4][2];
    const int q4 = (lane & 15) >> 2, pp = lane & 3, blk = (lane >> 4) & 1;
#define TR_LOAD(ks, ct) do { _Pragma("unroll") for (int t = 0; t < 2; ++t) \
      tf[ks][ct][t] = __builtin_amdgcn_ds_read_tr16_b64_v4i16((LAS s16x4*)(L + L_T + aw * 32768 + (ks) * 4096 + (8 * t + 4 * hi + q4) * 256 + \
          (((ct) ^ q4) << 6) + (((2 * blk + (pp >> 1)) ^ ((2 * t + hi) & 3)) << 4) + ((pp & 1) << 3))); } while (0)
    f32x16 sa;
#pragma unroll
    for (int i = 0; i < 16; ++i) sa[i] = 0.f;
    {
      v4u qf[4];
#pragma unroll
      for (int s = 0; s < 4; ++s) qf[s] = QRD(s);
      SB();
#pragma unroll
      for (int s = 0; s < 32; ++s) {
        sa = mfma32(ta[s], s < 28 ? qf[s & 3] : q7[s & 3], sa);
        if (s + 4 < 28) qf[s & 3] = QRD(s + 4);
        if (s >= 1) TR_LOAD(((s - 1) >> 1) & 7, ((s - 1) >> 4) * 2 + ((s - 1) & 1));
        SB();
      }
      TR_LOAD(7, 3);
    }
    float mx = sa[0];
#pragma unroll
    for (int i = 1; i < 16; ++i) mx = fmaxf(mx, sa[i]);
    mx = xor32_max(mx);
    *(LAS float*)(L + L_MX + r32 * 16 + aw * 4) = mx;
    lds_signal(ctl + C_MX);
    lds_wait(ctl + C_MX, 4);
    {
      const f32x4 m4 = *(const LAS f32x4*)(L + L_MX + r32 * 16);
      mx = fmaxf(fmaxf(m4[0], m4[1]), fmaxf(m4[2], m4[3]));
    }
    {
#pragma unroll
      for (int i = 0; i < 16; ++i) sa[i] = __builtin_amdgcn_exp2f(sa[i] - mx);
      *(LAS v4u*)(L + L_P + (2 * aw) * 1024 + lane * 16) = v4u{pk(sa[0], sa[1]), pk(sa[2], sa[3]), pk(sa[4], sa[5]), pk(sa[6], sa[7])};
      *(LAS v4u*)(L + L_P + (2 * aw + 1) * 1024 + lane * 16) = v4u{pk(sa[8], sa[9]), pk(sa[10], sa[11]), pk(sa[12], sa[13]), pk(sa[14], sa[15])};
      lds_signal(ctl + C_P);
      SB();
      float ps = (((sa[0] + sa[1]) + (sa[2] + sa[3])) + ((sa[4] + sa[5]) + (sa[6] + sa[7]))) +
                 (((sa[8] + sa[9]) + (sa[10] + sa[11])) + ((sa[12] + sa[13]) + (sa[14] + sa[15])));
      ps = xor32_sum(ps);
      *(LAS float*)(L + L_LS + r32 * 16 + aw * 4) = ps;
      lds_signal(ctl + C_LS);
    }
    lds_wait(ctl + C_P, 4);
    v4u pf[8];
#pragma unroll
    for (int ks = 0; ks < 8; ++ks) pf[ks] = *(const LAS v4u*)(L + L_P + ks * 1024 + lane * 16);
    f32x16 oa[4];
#pragma unroll
    for (int ct = 0; ct < 4; ++ct)
#pragma unroll
      for (int i = 0; i < 16; ++i) oa[ct][i] = 0.f;
#define PV_MFMA(ks, ct) oa[ct] = mfma32(__builtin_bit_cast(v4u, __builtin_shufflevector(tf[ks][ct][0], tf[ks][ct][1], 0, 1, 2, 3, 4, 5, 6, 7)), pf[ks], oa[ct])
#define O_WRITE(half, ct, gg) *(LAS v2u*)(L + L_T + aw * 32768 + (half) * 4096 + r32 * 128 + (((4 * ((ct) & 1) + (gg)) ^ (r32 & 7)) << 4) + hi * 8) = \
      v2u{pk(oa[ct][4 * (gg)], oa[ct][4 * (gg) + 1]), pk(oa[ct][4 * (gg) + 2], oa[ct][4 * (gg) + 3])}
    const unsigned ord = L_T + aw * 32768 + (lane >> 3) * 128 + (((lane & 7) ^ ((lane >> 3) & 7)) << 4);
    const unsigned ost = WS_OG + (oblk + ((lane >> 3) * 4 + aw) * 128) * 256 + (lane & 7) * 16;
#pragma unroll
    for (int ks = 0; ks < 8; ++ks) { PV_MFMA(ks, 0); PV_MFMA(ks, 1); }
    SB();
    {
      v4u ov[4];
#pragma unroll
      for (int ks = 0; ks < 8; ++ks) {
        PV_MFMA(ks, 2);
        if (ks >= 1 && ks <= 4) { O_WRITE(0, 0, ks - 1); }
        SB();
        PV_MFMA(ks, 3);
        if (ks >= 1 && ks <= 4) { O_WRITE(0, 1, ks - 1); }
        if (ks == 5) {
          CFENCE();
#pragma unroll
          for (int u = 0; u < 4; ++u) ov[u] = *(const LAS v4u*)(L + ord + u * 1024);
        }
        if (ks == 7) {
#pragma unroll
          for (int u = 0; u < 4; ++u) __builtin_amdgcn_raw_buffer_store_b128(ov[u], ws, (int)ost, u * 32 * 32768, SC1);
        }
        SB();
      }
    }
#pragma unroll
    for (int gg = 0; gg < 4; ++gg) { O_WRITE(1, 2, gg); O_WRITE(1, 3, gg); }
    CFENCE();
    {
      v4u ov[4];
#pragma unroll
      for (int u = 0; u < 4; ++u) ov[u] = *(const LAS v4u*)(L + ord + 4096 + u * 1024);
#pragma unroll
      for (int u = 0; u < 4; ++u) __builtin_amdgcn_raw_buffer_store_b128(ov[u], ws, (int)(ost + 128), u * 32 * 32768, SC1);
    }
    {
      lds_wait(ctl + C_LS, 4);
      const f32x4 l4 = *(const LAS f32x4*)(L + L_LS + r32 * 16);
      const float lsum = (l4[0] + l4[1]) + (l4[2] + l4[3]);
      wait_vm<0>();
      if (hi == 0) BST64((v2u{__float_as_uint(mx), __float_as_uint(lsum) | 1u}), ws, WS_F2 + (oblk + (r32 * 4 + aw) * 128) * 8);
    }
    {
      v4u st[4];
      __builtin_amdgcn_s_sleep(DS0);
      const unsigned fo = WS_F2 + blk1 * 1024 + aw * 256 + g * 64;
      for (unsigned spins = 0; spins < SPIN_CAP; ++spins) {
        CFENCE();
        const unsigned lv = BLD32(ws, WS_F2 + blk1 * 1024 + aw * 256 + (lane & 31) * 8 + 4, 0);
        if (__all(lv != 0u)) break;
        if ((spins & 7u) == 7u) HEARTBEAT(true);
        __builtin_amdgcn_s_sleep(DS);
      }
      CFENCE();
#pragma unroll
      for (int k = 0; k < 4; ++k) st[k] = BLD(ws, fo + k * 16, 0, SC1);
      v4u od[8];
#pragma unroll
      for (int i = 0; i < 8; ++i) od[i] = BLD(ws, (8 * g + i) * 256 + r * 16, WS_OG + blk1 * 32768 + aw * 8192, SC1);
      SB();
      lds_signal(ctl + C_OL);
      float m[8], l[8];
#pragma unroll
      for (int k = 0; k < 4; ++k) {
        m[2 * k] = __uint_as_float(st[k].x); l[2 * k] = __uint_as_float(st[k].y);
        m[2 * k + 1] = __uint_as_float(st[k].z); l[2 * k + 1] = __uint_as_float(st[k].w);
      }
      float M = m[0];
#pragma unroll
      for (int i = 1; i < 8; ++i) M = fmaxf(M, m[i]);
      M = xor32_max(xor16_max(M));
      float Ls = 0.f;
#pragma unroll
      for (int i = 0; i < 8; ++i) { m[i] = __builtin_amdgcn_exp2f(m[i] - M); Ls += m[i] * l[i]; }
      Ls = xor32_sum(xor16_sum(Ls));
      const float inv = 1.0f / Ls;
      v4u wf;
#pragma unroll
      for (int k = 0; k < 4; ++k) {
        const float w0 = m[2 * k] * inv, w1 = m[2 * k + 1] * inv;
        const unsigned hi2 = pk(w0, w1);
        const unsigned lo2 = pk(w0 - __uint_as_float(hi2 << 16), w1 - __uint_as_float(hi2 & 0xFFFF0000u));
        wf[k] = (r & 1) ? lo2 : hi2;
      }
      SB();
      f32x4 la[8];
#pragma unroll
      for (int e = 0; e < 8; ++e) la[e] = mfma16(wf, tcol(od, e), f32x4{0.f, 0.f, 0.f, 0.f});
      const v4u o = {pk(la[0][0] + la[0][1], la[1][0] + la[1][1]), pk(la[2][0] + la[2][1], la[3][0] + la[3][1]),
                     pk(la[4][0] + la[4][1], la[5][0] + la[5][1]), pk(la[6][0] + la[6][1], la[7][0] + la[7][1])};
      if (g == 0) *(LAS v4u*)(L + L_QA + aw * 256 + r * 16) = o;
      lds_signal(ctl + C_LAT);
    }
    if (aw == 0) {
      const int eb = h & 3, em = 4 * ((h >> 2) & 1) + (h >> 3);
      lds_wait_slow(ctl + C_WST, 4);
      v4u pv;
      const unsigned po = WS_X1 + (p1 * 128 + (lane >> 1) * 4 + cq) * 2048 + 1024 + eb * 256 + em * 32 + (lane & 1) * 16;
      for (unsigned spins = 0; spins < SPIN_CAP; ++spins) {
        CFENCE();
        pv = BLD(ws, po, 0, SC1);
        if (__all(pv.x != SENT && pv.y != SENT && pv.z != SENT && pv.w != SENT)) break;
        __builtin_amdgcn_s_sleep(1);
      }
      float s[8];
#pragma unroll
      for (int c = 0; c < 4; ++c) { s[2 * c] = __uint_as_float(pv[c] << 16); s[2 * c + 1] = __uint_as_float(pv[c] & 0xFFFF0000u); }
#pragma unroll
      for (int c = 0; c < 8; ++c) {
        float v = s[c];
        v = dpp_add<0x4E>(v);
        v = dpp_add<0x124>(v);
        v = dpp_add<0x128>(v);
        v = xor32_sum(xor16_sum(v));
        s[c] = v;
      }
      if (lane < 2) {
        const v4u o = {pk(s[0], s[1]), pk(s[2], s[3]), pk(s[4], s[5]), pk(s[6], s[7])};
        *(GAS v4u*)((char*)a_out + (eb * 1024 + p1 * 512 + cq * 128 + em * 16 + lane * 8) * 2) = o;
      }
    }
  } else {
    const int lw = wave - 4;
    if (lw == 0) {
      const unsigned lds0 = (unsigned)(size_t)L;
      const unsigned long long tk0 = __builtin_amdgcn_s_memtime();
      __builtin_amdgcn_s_setprio(3);
      {
        const GAS char* a = (const GAS char*)g_wk + (size_t)(128 * cq) * 8192 + h * 256;
        if (lane == 1) a = (const GAS char*)g_kv + ((size_t)b2 * 4096 + 128 * j) * 1024;
        if (lane == 2) a = (const GAS char*)g_wv + (size_t)(128 * cq) * 8192 + h * 256;
        if (lane == 3) a = (const GAS char*)g_wo + (size_t)(h * 128) * 1024 + cq * 256;
        if (lane < 4) {
          const unsigned t0 = __builtin_nontemporal_load((const GAS unsigned*)a);
          const unsigned t1 = BLD32(ws, lane == 0 ? WS_OG + blk1 * 32768 : WS_OG + p2 * 4194304 + ((lane - 1) << 21) - (lane == 3 ? 4 : 0), 0);
          asm volatile("" :: "v"(t0), "v"(t1));
        }
      }
      lds_wait(ctl + C_QP, 1);
      {
        const char* wkb = (const char*)g_wk + (size_t)(128 * cq) * 8192 + h * 256;
        const unsigned vk = r * 8192 + g * 16;
        for (int G = 0; G < 4; ++G) {
#pragma unroll
          for (int i = 0; i < 8; ++i) dma16(wkb + (size_t)(G * 2 + (i >> 2)) * 16 * 8192 + (i & 3) * 64, vk, lds0 + L_WK + (G * 8 + i) * 1024);
        }
        wait_vm<0>();
        lds_signal(ctl + C_WK);
        while (lds_peek(ctl + C_QST) < 1u && __builtin_amdgcn_s_memtime() - tk0 < (unsigned long long)TRES) __builtin_amdgcn_s_sleep(1);
        CFENCE();
      }
      const char* kvb = (const char*)g_kv + ((size_t)b2 * 4096 + 128 * j) * 1024;
      unsigned vo[4];
#pragma unroll
      for (int u3 = 0; u3 < 4; ++u3) vo[u3] = g * 1024 + ((r ^ ((g << 2) | u3)) << 4);
      for (int G = 0; G < 16; ++G) {
        wait_vm<KDEPTH - 8>();
        const int done = 8 * G - (KDEPTH - 8);
        if (G == 12) lds_wait_slow(ctl + C_QST, 1);
        if (G == PG) lds_signal(ctl + C_PN);
        if (done == 32) lds_signal(ctl + C_KV0);
        if (done == 64) lds_signal(ctl + C_KV1);
        if (done == 96) lds_signal(ctl + C_KV2);
        const int q = G >> 2, u0 = (G & 3) * 8;
        const char* gb = kvb + u0 * 4096 + q * 256;
        const unsigned lb = lds0 + L_T + q * 32768 + u0 * 1024;
#pragma unroll
        for (int i = 0; i < 8; ++i) dma16(gb + i * 4096, vo[i & 3], lb + i * 1024);
      }
      if (120 - (KDEPTH - 8) < 96) { wait_vm<32>(); lds_signal(ctl + C_KV2); }
      wait_vm<0>();
      lds_signal(ctl + C_KV3);
      __builtin_amdgcn_s_setprio(0);
    } else if (lw == 3) {
      {
        const v4u z = {0u, 0u, 0u, 0u}, sn = {SENT, SENT, SENT, SENT};
        BST(z, ws, WS_F2 + blk1 * 1024 + lane * 16);
        BST(sn, ws, WS_X1 + blk1 * 2048 + lane * 16);
        BST(sn, ws, WS_X1 + blk1 * 2048 + 1024 + lane * 16);
        wait_vm<0>();
        lds_signal(ctl + C_QA);
      }
    }
    asm volatile("" :: "v"(c0), "v"(c1), "v"(c2));
    const __amdgpu_buffer_rsrc_t rv = RSRC(g_wv, 512 * 4096 * 2);
    const __amdgpu_buffer_rsrc_t ro = RSRC(g_wo, 512 * 4096 * 2);
    v4u wv[8], wo[8];
    lds_wait_slow(ctl + C_KV3, 1);
    lds_wait_slow(ctl + C_QS, 4);
#pragma unroll
    for (int jj = 0; jj < 8; ++jj) wv[jj] = BLD(rv, g * 8 * 8192 + h * 256 + r * 16, (128 * cq + 32 * lw + jj) * 8192, NT);
    SB();
    v4u tv[8], to[8];
#pragma unroll
    for (int i = 0; i < 8; ++i) { tv[i] = tcol(wv, i); asm volatile("" : "+v"(tv[i])); }
    lds_wait_slow(ctl + C_OL, 4);
#pragma unroll
    for (int jj = 0; jj < 8; ++jj) wo[jj] = BLD(ro, g * 8 * 1024 + r * 16, (h * 128 + 32 * lw + jj) * 1024 + cq * 256, NT);
    SB();
    lds_wait_slow(ctl + C_LAT, 4);
    {
      const v4u lf = *(const LAS v4u*)(L + L_QA + (r >> 2) * 256 + lw * 64 + g * 16);
      f32x4 ca[8];
#pragma unroll
      for (int i = 0; i < 8; ++i) ca[i] = mfma16(lf, tv[i], f32x4{0.f, 0.f, 0.f, 0.f});
      *(LAS f32x4*)(L + L_RED + (lw * 4 + g) * 512 + r * 32) = f32x4{ca[0][0], ca[1][0], ca[2][0], ca[3][0]};
      *(LAS f32x4*)(L + L_RED + (lw * 4 + g) * 512 + r * 32 + 16) = f32x4{ca[4][0], ca[5][0], ca[6][0], ca[7][0]};
      lds_signal(ctl + C_CTX);
      lds_wait(ctl + C_CTX, 4);
      f32x2 s = *(const LAS f32x2*)(L + L_RED + lw * 512 + lane * 8);
#pragma unroll
      for (int k = 1; k < 4; ++k) s += *(const LAS f32x2*)(L + L_RED + (k * 4 + lw) * 512 + lane * 8);
      const unsigned cv = pk_ns(s[0], s[1]);
      BST32(cv, ws, WS_X1 + blk1 * 2048 + lw * 256 + lane * 4);
      *(LAS unsigned*)(L + L_QP + (cq * 4 + lw) * 272 + lane * 4) = cv;
    }
#pragma unroll
    for (int i = 0; i < 8; ++i) { to[i] = tcol(wo, i); asm volatile("" : "+v"(to[i])); }
    {
      unsigned xv[3];
      const unsigned xb = WS_X1 + (p1 * 128 + h * 4) * 2048 + lw * 256 + lane * 4;
      for (unsigned spins = 0; spins < SPIN_CAP; ++spins) {
        CFENCE();
#pragma unroll
        for (int k = 0; k < 3; ++k) xv[k] = BLD32(ws, xb, ((cq + 1 + k) & 3) * 2048);
        if (__all(xv[0] != SENT && xv[1] != SENT && xv[2] != SENT)) break;
        __builtin_amdgcn_s_sleep(1);
      }
#pragma unroll
      for (int k = 0; k < 3; ++k) *(LAS unsigned*)(L + L_QP + (((cq + 1 + k) & 3) * 4 + lw) * 272 + lane * 4) = xv[k];
    }
    lds_signal(ctl + C_X1);
    lds_wait(ctl + C_X1, 4);
    {
      const v4u cf = *(const LAS v4u*)(L + L_QP + ((r & 3) * 4 + (r >> 2)) * 272 + lw * 64 + g * 16);
      float o[8];
#pragma unroll
      for (int i = 0; i < 8; ++i) {
        const f32x4 a = mfma16(cf, to[i], f32x4{0.f, 0.f, 0.f, 0.f});
        o[i] = (a[0] + a[1]) + (a[2] + a[3]);
      }
      *(LAS f32x4*)(L + L_RED3 + (lw * 4 + g) * 512 + r * 32) = f32x4{o[0], o[1], o[2], o[3]};
      *(LAS f32x4*)(L + L_RED3 + (lw * 4 + g) * 512 + r * 32 + 16) = f32x4{o[4], o[5], o[6], o[7]};
      lds_signal(ctl + C_O3);
      lds_wait(ctl + C_O3, 4);
      f32x2 s = *(const LAS f32x2*)(L + L_RED3 + lw * 512 + lane * 8);
#pragma unroll
      for (int k = 1; k < 4; ++k) s += *(const LAS f32x2*)(L + L_RED3 + (k * 4 + lw) * 512 + lane * 8);
      BST32(pk_ns(s[0], s[1]), ws, WS_X1 + blk1 * 2048 + 1024 + lw * 256 + lane * 4);
    }
    lds_signal(ctl + C_WST);
  }
}

extern "C" void kernel_launch(void* const* d_in, const int* in_sizes, int n_in, void* d_out, int out_size, void* d_ws,
                              size_t ws_size, hipStream_t stream) {
  Params P;
  for (int p = 0; p < 2; ++p) {
    P.x[p] = d_in[6 * p + 0]; P.ckv[p] = d_in[6 * p + 1]; P.wq[p] = d_in[6 * p + 2];
    P.wuk[p] = d_in[6 * p + 3]; P.wuv[p] = d_in[6 * p + 4]; P.wo[p] = d_in[6 * p + 5];
  }
  P.out = d_out; P.ws = d_ws;
  (void)hipFuncSetAttribute((const void*)mla_fused, hipFuncAttributeMaxDynamicSharedMemorySize, (int)L_END);
  mla_fused<<<256, 512, L_END, stream>>>(P);
}
```

```cpp
#include <hip/hip_runtime.h>
#include <hip/hip_bf16.h>

#define GAS __attribute__((address_space(1)))
#define LAS __attribute__((address_space(3)))
typedef unsigned v4u __attribute__((ext_vector_type(4)));
typedef unsigned v2u __attribute__((ext_vector_type(2)));
typedef float f32x2 __attribute__((ext_vector_type(2)));
typedef float f32x4 __attribute__((ext_vector_type(4)));
typedef float f32x16 __attribute__((ext_vector_type(16)));
typedef short bf16x8 __attribute__((ext_vector_type(8)));
typedef short s16x4 __attribute__((ext_vector_type(4)));
#define RLX_WG __ATOMIC_RELAXED, __HIP_MEMORY_SCOPE_WORKGROUP
#define DI __device__ __forceinline__

constexpr int KDEPTH = 40;
constexpr int PG = 12;
constexpr int TRES = 7000;
constexpr int CODE_TAIL = 16;
#define XS0 24
#define XS 8
#define FS 16
#define DS0 16
#define DS 3

constexpr unsigned WS_F1 = 0;
constexpr unsigned WS_XG = 4096;
constexpr unsigned WS_QG = WS_XG + 64 * 4 * 2048;
constexpr unsigned WS_Q7 = WS_QG + 8 * 32768;
constexpr unsigned WS_F2 = WS_Q7 + 8 * 4096;
constexpr unsigned WS_X1 = WS_F2 + 256 * 1024;
constexpr unsigned WS_OG = WS_X1 + 256 * 2048;
constexpr unsigned WS_END = WS_OG + 256 * 32768;

constexpr unsigned L_T = 0;
constexpr unsigned L_WK = 3 * 32768;
constexpr unsigned L_CT = 131072;
constexpr unsigned L_MX = L_CT + 128;
constexpr unsigned L_QA = L_MX + 512;
constexpr unsigned L_SG = L_QA + 1024;
constexpr unsigned L_RED = L_SG;
constexpr unsigned L_QP = L_SG + 8192;
constexpr unsigned L_P = L_SG + 16384;
constexpr unsigned L_RED3 = L_SG + 16384;
constexpr unsigned L_LS = L_SG + 28 * 1024;
constexpr unsigned L_END = L_LS + 512;
enum { C_QP = 0, C_QB, C_QA, C_F1, C_QS, C_KV0, C_KV1, C_KV2, C_KV3, C_MX, C_P, C_LS,
       C_LAT, C_CTX, C_WST, C_WK, C_QST, C_O3, C_PN, C_OL, C_N };

constexpr unsigned SENT = 0xFFFFFFFFu;
constexpr unsigned SPIN_CAP = 1u << 16;
constexpr unsigned LDS_CAP = 1u << 22;
constexpr float QSCALE = 0.08838834764831845f * 1.4426950408889634f;

struct Params {
  const void* x[2]; const void* ckv[2]; const void* wq[2]; const void* wuk[2]; const void* wuv[2]; const void* wo[2];
  void* out; void* ws;
};

DI f32x4 mfma16(v4u a, v4u b, f32x4 c) {
  return __builtin_amdgcn_mfma_f32_16x16x32_bf16(__builtin_bit_cast(bf16x8, a), __builtin_bit_cast(bf16x8, b), c, 0, 0, 0);
}
DI f32x16 mfma32(v4u a, v4u b, f32x16 c) {
  return __builtin_amdgcn_mfma_f32_32x32x16_bf16(__builtin_bit_cast(bf16x8, a), __builtin_bit_cast(bf16x8, b), c, 0, 0, 0);
}
typedef __bf16 bf2_t __attribute__((ext_vector_type(2)));
DI unsigned pk(float lo, float hi) {
  f32x2 f = {lo, hi};
  return __builtin_bit_cast(unsigned, __builtin_convertvector(f, bf2_t));
}
DI unsigned pk_ns(float lo, float hi) {
  const unsigned u = pk(lo, hi);
  return (u == SENT) ? 0x7FC07FC0u : u;
}
DI v4u tcol(const v4u (&w)[8], int i) {
  const unsigned sel = (i & 1) ? 0x07060302u : 0x05040100u;
  v4u o;
  o.x = __builtin_amdgcn_perm(w[1][i >> 1], w[0][i >> 1], sel);
  o.y = __builtin_amdgcn_perm(w[3][i >> 1], w[2][i >> 1], sel);
  o.z = __builtin_amdgcn_perm(w[5][i >> 1], w[4][i >> 1], sel);
  o.w = __builtin_amdgcn_perm(w[7][i >> 1], w[6][i >> 1], sel);
  return o;
}
#define CFENCE() asm volatile("" ::: "memory")
#define SB() __builtin_amdgcn_sched_barrier(0)
DI void lds_signal(LAS unsigned* c) {
  unsigned long long keep;
  asm volatile("s_mov_b64 %0, exec\n\ts_mov_b64 exec, 1\n\tds_add_u32 %1, %2\n\ts_mov_b64 exec, %0"
               : "=&s"(keep) : "v"((unsigned)(size_t)c), "v"(1u) : "memory");
}
DI unsigned lds_peek(LAS unsigned* c) { return (unsigned)__builtin_amdgcn_readfirstlane(__hip_atomic_load(c, RLX_WG)); }
DI void lds_wait_slow(LAS unsigned* c, unsigned target) {
  CFENCE();
  for (unsigned n = 0; n < LDS_CAP && lds_peek(c) < target; ++n) __builtin_amdgcn_s_sleep(1);
  CFENCE();
}
DI void lds_wait(LAS unsigned* c, unsigned target) {
  CFENCE();
  for (unsigned n = 0; n < LDS_CAP && lds_peek(c) < target; ++n) {}
  CFENCE();
}
DI float xor32_max(float v) {
  auto r = __builtin_amdgcn_permlane32_swap(__float_as_uint(v), __float_as_uint(v), false, false);
  return fmaxf(__uint_as_float(r[0]), __uint_as_float(r[1]));
}
DI float xor32_sum(float v) {
  auto r = __builtin_amdgcn_permlane32_swap(__float_as_uint(v), __float_as_uint(v), false, false);
  return __uint_as_float(r[0]) + __uint_as_float(r[1]);
}
DI float xor16_max(float v) {
  auto r = __builtin_amdgcn_permlane16_swap(__float_as_uint(v), __float_as_uint(v), false, false);
  return fmaxf(__uint_as_float(r[0]), __uint_as_float(r[1]));
}
DI float xor16_sum(float v) {
  auto r = __builtin_amdgcn_permlane16_swap(__float_as_uint(v), __float_as_uint(v), false, false);
  return __uint_as_float(r[0]) + __uint_as_float(r[1]);
}
template <int CTRL> DI float dpp_add(float v) {
  return v + __uint_as_float(__builtin_amdgcn_update_dpp(0u, __float_as_uint(v), CTRL, 0xF, 0xF, false));
}
DI void dma16(const void* sbase, unsigned voff, unsigned lds_dst) {
  unsigned keep;
  asm volatile("s_mov_b32 %0, m0\n\ts_mov_b32 m0, %3\n\ts_nop 0\n\tglobal_load_lds_dwordx4 %1, %2 nt\n\ts_mov_b32 m0, %0"
               : "=&s"(keep) : "v"(voff), "s"(sbase), "s"(lds_dst) : "memory");
}
template <int N> DI void wait_vm() { asm volatile("s_waitcnt vmcnt(%0)" ::"i"(N) : "memory"); }
#define RSRC(ptr, bytes) __builtin_amdgcn_make_buffer_rsrc((void*)(ptr), 0, (int)(bytes), 0x00020000)
#define BLD(rs, vo, so, aux) __builtin_amdgcn_raw_buffer_load_b128(rs, (int)(vo), (int)(so), aux)
#define BLD64(rs, vo, so) __builtin_amdgcn_raw_buffer_load_b64(rs, (int)(vo), (int)(so), SC1)
#define BLD32(rs, vo, so) __builtin_amdgcn_raw_buffer_load_b32(rs, (int)(vo), (int)(so), SC1)
#define BST(v, rs, vo) __builtin_amdgcn_raw_buffer_store_b128(v, rs, (int)(vo), 0, SC1)
#define BST64(v, rs, vo) __builtin_amdgcn_raw_buffer_store_b64(v, rs, (int)(vo), 0, SC1)
#define BST32(v, rs, vo) __builtin_amdgcn_raw_buffer_store_b32(v, rs, (int)(vo), 0, SC1)
constexpr int SC1 = 16, NT = 2;

__global__ void __launch_bounds__(512, 2) mla_fused(Params P) {
  extern __shared__ __attribute__((aligned(16))) unsigned char smem[];
  LAS unsigned char* L = (LAS unsigned char*)smem;
  LAS unsigned* ctl = (LAS unsigned*)(L + L_CT);
  const int tid = threadIdx.x, lane = tid & 63;
  const int wave = __builtin_amdgcn_readfirstlane(tid >> 6);
  const int w = blockIdx.x;
  const int ph = w & 63, p1 = ph >> 5, h = w & 31, cq = w >> 6;
  const int src = h * 4 + cq;
  const int blk1 = p1 * 128 + src;
  const int pb = w & 7, p2 = pb >> 2, b2 = pb & 3, j = w >> 3;
  const unsigned oblk = p2 * 128 * 128 + b2 * 32 + j;
  const int r = lane & 15, g = lane >> 4;
  unsigned c0 = 0u, c1 = 0u, c2 = 0u;
  if (wave == 5 && j < 4) {
    unsigned long long pc;
    asm volatile("s_getpc_b64 %0" : "=s"(pc));
    const GAS char* cb = (const GAS char*)(pc & ~0xFFull) + lane * 128;
    c0 = *(const GAS unsigned*)(cb);
    c1 = *(const GAS unsigned*)(cb + 8192);
    c2 = *(const GAS unsigned*)(cb + (lane < CODE_TAIL ? 16384 : 0));
  }
  const void *a_x0 = P.x[0], *a_x1 = P.x[1], *a_kv0 = P.ckv[0], *a_kv1 = P.ckv[1], *a_q0 = P.wq[0], *a_q1 = P.wq[1], *a_k0 = P.wuk[0],
             *a_k1 = P.wuk[1], *a_v0 = P.wuv[0], *a_v1 = P.wuv[1], *a_o0 = P.wo[0], *a_o1 = P.wo[1];
  void *a_out = P.out, *a_ws = P.ws;
  asm volatile("" : "+s"(a_x0), "+s"(a_x1), "+s"(a_kv0), "+s"(a_kv1), "+s"(a_q0), "+s"(a_q1), "+s"(a_k0), "+s"(a_k1), "+s"(a_v0), "+s"(a_v1),
                    "+s"(a_o0), "+s"(a_o1), "+s"(a_out), "+s"(a_ws));
  const void* g_x = p1 ? a_x1 : a_x0;
  const void* g_kv = p2 ? a_kv1 : a_kv0;
  const void* g_wq = p1 ? a_q1 : a_q0;
  const void* g_wk = p1 ? a_k1 : a_k0;
  const void* g_wv = p1 ? a_v1 : a_v0;
  const void* g_wo = p1 ? a_o1 : a_o0;
  const __amdgpu_buffer_rsrc_t ws = RSRC(a_ws, WS_END);
  const unsigned xo = WS_XG + ph * 8192 + tid * 8;
  const unsigned f1own = WS_F1 + ((p1 * 4 + (lane & 3)) * 128 + src) * 4;
  unsigned xtag = 0u, xs[3] = {0u, 0u, 0u}, f1tag = 0u;
  v2u f1s = {0u, 0u};
  v4u xf, wq[8];
  if (wave < 4) {
    const int aw = wave;
    const __amdgpu_buffer_rsrc_t rx = RSRC(g_x, 4 * 512 * 2);
    const __amdgpu_buffer_rsrc_t rq = RSRC(g_wq, 512 * 4096 * 2);
    xtag = BLD32(ws, xo + cq * 2048 + 4, 0);
#pragma unroll
    for (int k = 0; k < 3; ++k) xs[k] = BLD32(ws, xo + 4, ((cq + 1 + k) & 3) * 2048);
    f1s = BLD64(ws, WS_F1 + pb * 512 + lane * 8, 0);
    f1tag = BLD32(ws, f1own, 0);
    SB();
#pragma unroll
    for (int jj = 0; jj < 8; ++jj) wq[jj] = BLD(rq, g * 8 * 8192 + h * 256 + r * 16, (128 * cq + 32 * aw + jj) * 8192, NT);
    xf = BLD(rx, (r >> 2) * 1024 + g * 16, (128 * cq + 32 * aw) * 2, 0);
    SB();
  }
  if (tid < C_N) ctl[tid] = 0u;
  asm volatile("s_waitcnt lgkmcnt(0)" ::: "memory");
  __builtin_amdgcn_s_barrier();

  if (wave < 4) {
    const int aw = wave;
    __builtin_amdgcn_s_setprio(1);
    unsigned xval;
    {
      f32x4 acc[8];
#pragma unroll
      for (int i = 0; i < 8; ++i) acc[i] = mfma16(xf, tcol(wq, i), f32x4{0.f, 0.f, 0.f, 0.f});
      *(LAS f32x4*)(L + L_RED + (aw * 4 + g) * 512 + r * 32) = f32x4{acc[0][0], acc[1][0], acc[2][0], acc[3][0]};
      *(LAS f32x4*)(L + L_RED + (aw * 4 + g) * 512 + r * 32 + 16) = f32x4{acc[4][0], acc[5][0], acc[6][0], acc[7][0]};
      lds_signal(ctl + C_QP);
      lds_wait(ctl + C_QP, 4);
      f32x2 s = *(const LAS f32x2*)(L + L_RED + aw * 512 + lane * 8);
#pragma unroll
      for (int k = 1; k < 4; ++k) s += *(const LAS f32x2*)(L + L_RED + (k * 4 + aw) * 512 + lane * 8);
      xval = pk(s[0], s[1]);
    }
    xtag += 1u;
    BST64((v2u{xval, xtag}), ws, xo + cq * 2048);
    *(LAS unsigned*)(L + L_QP + (cq * 4 + aw) * 272 + lane * 4) = xval;
#define HEARTBEAT(flag_lanes) do { xtag += 1u; BST64((v2u{xval, xtag}), ws, xo + cq * 2048); \
      f1tag += 1u; if (flag_lanes) BST32(f1tag, ws, f1own); } while (0)
    {
      v2u xv[3];
      __builtin_amdgcn_s_sleep(XS0);
      for (unsigned spins = 0; spins < SPIN_CAP; ++spins) {
        CFENCE();
#pragma unroll
        for (int k = 0; k < 3; ++k) xv[k] = BLD64(ws, xo, ((cq + 1 + k) & 3) * 2048);
        if (__all(xv[0].y != xs[0] && xv[1].y != xs[1] && xv[2].y != xs[2])) break;
        if ((spins & 3u) == 3u) HEARTBEAT(false);
        __builtin_amdgcn_s_sleep(XS);
      }
#pragma unroll
      for (int k = 0; k < 3; ++k) *(LAS unsigned*)(L + L_QP + (((cq + 1 + k) & 3) * 4 + aw) * 272 + lane * 4) = xv[k].x;
    }
    lds_signal(ctl + C_QB);
    {
      lds_wait(ctl + C_WK, 1);
      v4u wk[2][4];
#pragma unroll
      for (int t = 0; t < 2; ++t)
#pragma unroll
        for (int s = 0; s < 4; ++s) wk[t][s] = *(const LAS v4u*)(L + L_WK + ((aw * 2 + t) * 4 + s) * 1024 + lane * 16);
      lds_wait(ctl + C_QB, 4);
      f32x4 a2[2] = {f32x4{0.f, 0.f, 0.f, 0.f}, f32x4{0.f, 0.f, 0.f, 0.f}};
#pragma unroll
      for (int s = 0; s < 4; ++s) {
        const v4u qf = *(const LAS v4u*)(L + L_QP + ((r & 3) * 4 + (r >> 2)) * 272 + s * 64 + g * 16);
        a2[0] = mfma16(qf, wk[0][s], a2[0]);
        a2[1] = mfma16(qf, wk[1][s], a2[1]);
      }
#pragma unroll
      for (int t = 0; t < 2; ++t) {
        const float v = ((a2[t][0] + a2[t][1]) + (a2[t][2] + a2[t][3])) * QSCALE;
        *(LAS unsigned short*)(L + L_QA + g * 256 + (32 * aw + 16 * t + r) * 2) = (unsigned short)(pk(v, v) & 0xFFFFu);
      }
    }
    lds_signal(ctl + C_QA);
    if (aw == 0) {
      lds_wait(ctl + C_QA, 5);
      const v4u v = *(const LAS v4u*)(L + L_QA + lane * 16);
      BST(v, ws, (cq == 3 && r >= 8) ? WS_Q7 + (((p1 * 4 + g) * 4 + ((r - 8) >> 1)) * 64 + (r & 1) * 32 + h) * 16
                                     : WS_QG + ((p1 * 4 + g) * 32 + h) * 1024 + cq * 256 + r * 16);
      lds_signal(ctl + C_QST);
      wait_vm<0>();
      f1tag += 1u;
      if (lane < 4) BST32(f1tag, ws, f1own);
    }
    const int r32 = lane & 31, hi = lane >> 5;
    v4u ta[32];
    const unsigned tbase = L_T + (32 * aw + r32) * 256;
    const unsigned tswz = ((r32 & 3) << 2) | ((r32 >> 2) & 3);
#define TA_LOAD(q) do { _Pragma("unroll") for (int s8 = 0; s8 < 8; ++s8) \
      ta[8 * (q) + s8] = *(const LAS v4u*)(L + tbase + (q) * 32768 + (((2 * s8 + hi) ^ tswz) << 4)); SB(); } while (0)
    lds_wait_slow(ctl + C_KV0, 1);
    TA_LOAD(0);
    lds_wait_slow(ctl + C_KV1, 1);
    TA_LOAD(1);
    if (aw == 0) {
      bool c0 = false, c1 = false;
      lds_wait_slow(ctl + C_PN, 1);
      for (unsigned spins = 0; spins < SPIN_CAP; ++spins) {
        CFENCE();
        const v2u f = BLD64(ws, WS_F1 + pb * 512 + lane * 8, 0);
        c0 = c0 || f.x != f1s.x; c1 = c1 || f.y != f1s.y;
        if (__all(c0 && c1)) break;
        if ((spins & 3u) == 3u) HEARTBEAT(lane < 4);
        __builtin_amdgcn_s_sleep(FS);
      }
      lds_signal(ctl + C_F1);
    } else {
      CFENCE();
      for (unsigned spins = 0; spins < LDS_CAP && lds_peek(ctl + C_F1) < 1u; ++spins) {
        __builtin_amdgcn_s_sleep(1);
        if ((spins & 127u) == 127u) HEARTBEAT(false);
      }
      CFENCE();
    }
#define QRD(s) (*(const LAS v4u*)(L + L_SG + ((s) >> 2) * 4096 + r32 * 128 + (((2 * ((s) & 3) + hi) ^ ((r32 >> 1) & 7)) << 4)))
    v4u q7[4];
    {
      const int qrow = 8 * aw + (lane >> 3);
      v4u sg[7];
#pragma unroll
      for (int s = 0; s < 7; ++s) sg[s] = BLD(ws, qrow * 1024 + (lane & 7) * 16 + s * 128, WS_QG + pb * 32768, SC1);
#pragma unroll
      for (int s = 0; s < 4; ++s) q7[s] = BLD(ws, lane * 16 + s * 1024, WS_Q7 + pb * 4096, SC1);
      SB();
      lds_wait_slow(ctl + C_KV2, 1);
      TA_LOAD(2);
#pragma unroll
      for (int s = 0; s < 7; ++s) *(LAS v4u*)(L + L_SG + s * 4096 + qrow * 128 + (((lane & 7) ^ ((qrow >> 1) & 7)) << 4)) = sg[s];
      lds_signal(ctl + C_QS);
    }
    lds_wait(ctl + C_KV3, 1);
    TA_LOAD(3);
    lds_wait(ctl + C_QS, 4);
    s16x4 tf[8][4][2];
    const int q4 = (lane & 15) >> 2, pp = lane & 3, blk = (lane >> 4) & 1;
#define TR_LOAD(ks, ct) do { _Pragma("unroll") for (int t = 0; t < 2; ++t) \
      tf[ks][ct][t] = __builtin_amdgcn_ds_read_tr16_b64_v4i16((LAS s16x4*)(L + L_T + aw * 32768 + (ks) * 4096 + (8 * t + 4 * hi + q4) * 256 + \
          (((ct) ^ q4) << 6) + (((2 * blk + (pp >> 1)) ^ ((2 * t + hi) & 3)) << 4) + ((pp & 1) << 3))); } while (0)
    f32x16 sa;
#pragma unroll
    for (int i = 0; i < 16; ++i) sa[i] = 0.f;
    {
      v4u qf[4];
#pragma unroll
      for (int s = 0; s < 4; ++s) qf[s] = QRD(s);
      SB();
#pragma unroll
      for (int s = 0; s < 32; ++s) {
        sa = mfma32(ta[s], s < 28 ? qf[s & 3] : q7[s & 3], sa);
        if (s + 4 < 28) qf[s & 3] = QRD(s + 4);
        if (s >= 1) TR_LOAD(((s - 1) >> 1) & 7, ((s - 1) >> 4) * 2 + ((s - 1) & 1));
        SB();
      }
      TR_LOAD(7, 3);
    }
    float mx = sa[0];
#pragma unroll
    for (int i = 1; i < 16; ++i) mx = fmaxf(mx, sa[i]);
    mx = xor32_max(mx);
    *(LAS float*)(L + L_MX + r32 * 16 + aw * 4) = mx;
    lds_signal(ctl + C_MX);
    lds_wait(ctl + C_MX, 4);
    {
      const f32x4 m4 = *(const LAS f32x4*)(L + L_MX + r32 * 16);
      mx = fmaxf(fmaxf(m4[0], m4[1]), fmaxf(m4[2], m4[3]));
    }
    {
#pragma unroll
      for (int i = 0; i < 16; ++i) sa[i] = __builtin_amdgcn_exp2f(sa[i] - mx);
      *(LAS v4u*)(L + L_P + (2 * aw) * 1024 + lane * 16) = v4u{pk(sa[0], sa[1]), pk(sa[2], sa[3]), pk(sa[4], sa[5]), pk(sa[6], sa[7])};
      *(LAS v4u*)(L + L_P + (2 * aw + 1) * 1024 + lane * 16) = v4u{pk(sa[8], sa[9]), pk(sa[10], sa[11]), pk(sa[12], sa[13]), pk(sa[14], sa[15])};
      lds_signal(ctl + C_P);
      SB();
      float ps = (((sa[0] + sa[1]) + (sa[2] + sa[3])) + ((sa[4] + sa[5]) + (sa[6] + sa[7]))) +
                 (((sa[8] + sa[9]) + (sa[10] + sa[11])) + ((sa[12] + sa[13]) + (sa[14] + sa[15])));
      ps = xor32_sum(ps);
      *(LAS float*)(L + L_LS + r32 * 16 + aw * 4) = ps;
      lds_signal(ctl + C_LS);
    }
    lds_wait(ctl + C_P, 4);
    v4u pf[8];
#pragma unroll
    for (int ks = 0; ks < 8; ++ks) pf[ks] = *(const LAS v4u*)(L + L_P + ks * 1024 + lane * 16);
    f32x16 oa[4];
#pragma unroll
    for (int ct = 0; ct < 4; ++ct)
#pragma unroll
      for (int i = 0; i < 16; ++i) oa[ct][i] = 0.f;
#define PV_MFMA(ks, ct) oa[ct] = mfma32(__builtin_bit_cast(v4u, __builtin_shufflevector(tf[ks][ct][0], tf[ks][ct][1], 0, 1, 2, 3, 4, 5, 6, 7)), pf[ks], oa[ct])
#define O_WRITE(half, ct, gg) *(LAS v2u*)(L + L_T + aw * 32768 + (half) * 4096 + r32 * 128 + (((4 * ((ct) & 1) + (gg)) ^ (r32 & 7)) << 4) + hi * 8) = \
      v2u{pk(oa[ct][4 * (gg)], oa[ct][4 * (gg) + 1]), pk(oa[ct][4 * (gg) + 2], oa[ct][4 * (gg) + 3])}
    const unsigned ord = L_T + aw * 32768 + (lane >> 3) * 128 + (((lane & 7) ^ ((lane >> 3) & 7)) << 4);
    const unsigned ost = WS_OG + (oblk + ((lane >> 3) * 4 + aw) * 128) * 256 + (lane & 7) * 16;
#pragma unroll
    for (int ks = 0; ks < 8; ++ks) { PV_MFMA(ks, 0); PV_MFMA(ks, 1); }
    SB();
    {
      v4u ov[4];
#pragma unroll
      for (int ks = 0; ks < 8; ++ks) {
        PV_MFMA(ks, 2);
        if (ks >= 1 && ks <= 4) { O_WRITE(0, 0, ks - 1); }
        SB();
        PV_MFMA(ks, 3);
        if (ks >= 1 && ks <= 4) { O_WRITE(0, 1, ks - 1); }
        if (ks == 5) {
          CFENCE();
#pragma unroll
          for (int u = 0; u < 4; ++u) ov[u] = *(const LAS v4u*)(L + ord + u * 1024);
        }
        if (ks == 7) {
#pragma unroll
          for (int u = 0; u < 4; ++u) __builtin_amdgcn_raw_buffer_store_b128(ov[u], ws, (int)ost, u * 32 * 32768, SC1);
        }
        SB();
      }
    }
#pragma unroll
    for (int gg = 0; gg < 4; ++gg) { O_WRITE(1, 2, gg); O_WRITE(1, 3, gg); }
    CFENCE();
    {
      v4u ov[4];
#pragma unroll
      for (int u = 0; u < 4; ++u) ov[u] = *(const LAS v4u*)(L + ord + 4096 + u * 1024);
#pragma unroll
      for (int u = 0; u < 4; ++u) __builtin_amdgcn_raw_buffer_store_b128(ov[u], ws, (int)(ost + 128), u * 32 * 32768, SC1);
    }
    {
      lds_wait(ctl + C_LS, 4);
      const f32x4 l4 = *(const LAS f32x4*)(L + L_LS + r32 * 16);
      const float lsum = (l4[0] + l4[1]) + (l4[2] + l4[3]);
      wait_vm<0>();
      if (hi == 0) BST64((v2u{__float_as_uint(mx), __float_as_uint(lsum) | 1u}), ws, WS_F2 + (oblk + (r32 * 4 + aw) * 128) * 8);
    }
    {
      v4u st[4];
      __builtin_amdgcn_s_sleep(DS0);
      const unsigned fo = WS_F2 + blk1 * 1024 + aw * 256 + g * 64;
      for (unsigned spins = 0; spins < SPIN_CAP; ++spins) {
        CFENCE();
        const unsigned lv = BLD32(ws, WS_F2 + blk1 * 1024 + aw * 256 + (lane & 31) * 8 + 4, 0);
        if (__all(lv != 0u)) break;
        if ((spins & 7u) == 7u) HEARTBEAT(lane == aw);
        __builtin_amdgcn_s_sleep(DS);
      }
      CFENCE();
#pragma unroll
      for (int k = 0; k < 4; ++k) st[k] = BLD(ws, fo + k * 16, 0, SC1);
      v4u od[8];
#pragma unroll
      for (int i = 0; i < 8; ++i) od[i] = BLD(ws, (8 * g + i) * 256 + r * 16, WS_OG + blk1 * 32768 + aw * 8192, SC1);
      SB();
      lds_signal(ctl + C_OL);
      float m[8], l[8];
#pragma unroll
      for (int k = 0; k < 4; ++k) {
        m[2 * k] = __uint_as_float(st[k].x); l[2 * k] = __uint_as_float(st[k].y);
        m[2 * k + 1] = __uint_as_float(st[k].z); l[2 * k + 1] = __uint_as_float(st[k].w);
      }
      float M = m[0];
#pragma unroll
      for (int i = 1; i < 8; ++i) M = fmaxf(M, m[i]);
      M = xor32_max(xor16_max(M));
      float Ls = 0.f;
#pragma unroll
      for (int i = 0; i < 8; ++i) { m[i] = __builtin_amdgcn_exp2f(m[i] - M); Ls += m[i] * l[i]; }
      Ls = xor32_sum(xor16_sum(Ls));
      const float inv = 1.0f / Ls;
      v4u wf;
#pragma unroll
      for (int k = 0; k < 4; ++k) {
        const float w0 = m[2 * k] * inv, w1 = m[2 * k + 1] * inv;
        const unsigned hi2 = pk(w0, w1);
        const unsigned lo2 = pk(w0 - __uint_as_float(hi2 << 16), w1 - __uint_as_float(hi2 & 0xFFFF0000u));
        wf[k] = (r & 1) ? lo2 : hi2;
      }
      SB();
      f32x4 la[8];
#pragma unroll
      for (int e = 0; e < 8; ++e) la[e] = mfma16(wf, tcol(od, e), f32x4{0.f, 0.f, 0.f, 0.f});
      const v4u o = {pk(la[0][0] + la[0][1], la[1][0] + la[1][1]), pk(la[2][0] + la[2][1], la[3][0] + la[3][1]),
                     pk(la[4][0] + la[4][1], la[5][0] + la[5][1]), pk(la[6][0] + la[6][1], la[7][0] + la[7][1])};
      if (g == 0) *(LAS v4u*)(L + L_QA + aw * 256 + r * 16) = o;
      lds_signal(ctl + C_LAT);
    }
    if (aw == 0) {
      const int eb = h & 3, em = 4 * ((h >> 2) & 1) + (h >> 3);
      lds_wait_slow(ctl + C_WST, 4);
      v4u pv;
      const unsigned po = WS_X1 + (p1 * 128 + (lane >> 1) * 4 + cq) * 2048 + 1024 + eb * 256 + em * 32 + (lane & 1) * 16;
      for (unsigned spins = 0; spins < SPIN_CAP; ++spins) {
        CFENCE();
        pv = BLD(ws, po, 0, SC1);
        if (__all(pv.x != SENT && pv.y != SENT && pv.z != SENT && pv.w != SENT)) break;
        __builtin_amdgcn_s_sleep(1);
      }
      float s[8];
#pragma unroll
      for (int c = 0; c < 4; ++c) { s[2 * c] = __uint_as_float(pv[c] << 16); s[2 * c + 1] = __uint_as_float(pv[c] & 0xFFFF0000u); }
#pragma unroll
      for (int c = 0; c < 8; ++c) {
        float v = s[c];
        v = dpp_add<0x4E>(v);
        v = dpp_add<0x124>(v);
        v = dpp_add<0x128>(v);
        v = xor32_sum(xor16_sum(v));
        s[c] = v;
      }
      if (lane < 2) {
        const v4u o = {pk(s[0], s[1]), pk(s[2], s[3]), pk(s[4], s[5]), pk(s[6], s[7])};
        *(GAS v4u*)((char*)a_out + (eb * 1024 + p1 * 512 + cq * 128 + em * 16 + lane * 8) * 2) = o;
      }
    }
  } else {
    const int lw = wave - 4;
    if (lw == 0) {
      const unsigned lds0 = (unsigned)(size_t)L;
      const unsigned long long tk0 = __builtin_amdgcn_s_memtime();
      __builtin_amdgcn_s_setprio(3);
      {
        const GAS char* a = (const GAS char*)g_wk + (size_t)(128 * cq) * 8192 + h * 256;
        if (lane == 1) a = (const GAS char*)g_kv + ((size_t)b2 * 4096 + 128 * j) * 1024;
        if (lane == 2) a = (const GAS char*)g_wv + (size_t)(128 * cq) * 8192 + h * 256;
        if (lane == 3) a = (const GAS char*)g_wo + (size_t)(h * 128) * 1024 + cq * 256;
        if (lane < 4) {
          const unsigned t0 = __builtin_nontemporal_load((const GAS unsigned*)a);
          const unsigned t1 = BLD32(ws, lane == 0 ? WS_OG + blk1 * 32768 : WS_OG + p2 * 4194304 + ((lane - 1) << 21) - (lane == 3 ? 4 : 0), 0);
          asm volatile("" :: "v"(t0), "v"(t1));
        }
      }
      lds_wait(ctl + C_QP, 1);
      {
        const char* wkb = (const char*)g_wk + (size_t)(128 * cq) * 8192 + h * 256;
        const unsigned vk = r * 8192 + g * 16;
        for (int G = 0; G < 4; ++G) {
#pragma unroll
          for (int i = 0; i < 8; ++i) dma16(wkb + (size_t)(G * 2 + (i >> 2)) * 16 * 8192 + (i & 3) * 64, vk, lds0 + L_WK + (G * 8 + i) * 1024);
        }
        wait_vm<0>();
        lds_signal(ctl + C_WK);
        while (lds_peek(ctl + C_QST) < 1u && __builtin_amdgcn_s_memtime() - tk0 < (unsigned long long)TRES) __builtin_amdgcn_s_sleep(1);
        CFENCE();
      }
      const char* kvb = (const char*)g_kv + ((size_t)b2 * 4096 + 128 * j) * 1024;
      unsigned vo[4];
#pragma unroll
      for (int u3 = 0; u3 < 4; ++u3) vo[u3] = g * 1024 + ((r ^ ((g << 2) | u3)) << 4);
      for (int G = 0; G < 16; ++G) {
        wait_vm<KDEPTH - 8>();
        const int done = 8 * G - (KDEPTH - 8);
        if (G == 12) lds_wait_slow(ctl + C_QST, 1);
        if (G == PG) lds_signal(ctl + C_PN);
        if (done == 32) lds_signal(ctl + C_KV0);
        if (done == 64) lds_signal(ctl + C_KV1);
        if (done == 96) lds_signal(ctl + C_KV2);
        const int q = G >> 2, u0 = (G & 3) * 8;
        const char* gb = kvb + u0 * 4096 + q * 256;
        const unsigned lb = lds0 + L_T + q * 32768 + u0 * 1024;
#pragma unroll
        for (int i = 0; i < 8; ++i) dma16(gb + i * 4096, vo[i & 3], lb + i * 1024);
      }
      if (120 - (KDEPTH - 8) < 96) { wait_vm<32>(); lds_signal(ctl + C_KV2); }
      wait_vm<0>();
      lds_signal(ctl + C_KV3);
      __builtin_amdgcn_s_setprio(0);
    } else if (lw == 3) {
      {
        const v4u z = {0u, 0u, 0u, 0u}, sn = {SENT, SENT, SENT, SENT};
        BST(z, ws, WS_F2 + blk1 * 1024 + lane * 16);
        BST(sn, ws, WS_X1 + blk1 * 2048 + lane * 16);
        BST(sn, ws, WS_X1 + blk1 * 2048 + 1024 + lane * 16);
        wait_vm<0>();
        lds_signal(ctl + C_QA);
      }
    }
    asm volatile("" :: "v"(c0), "v"(c1), "v"(c2));
    const __amdgpu_buffer_rsrc_t rv = RSRC(g_wv, 512 * 4096 * 2);
    const __amdgpu_buffer_rsrc_t ro = RSRC(g_wo, 512 * 4096 * 2);
    v4u wv[8], wo[8];
    lds_wait_slow(ctl + C_KV3, 1);
    lds_wait_slow(ctl + C_QS, 4);
#pragma unroll
    for (int jj = 0; jj < 8; ++jj) wv[jj] = BLD(rv, g * 8 * 8192 + h * 256 + r * 16, (128 * cq + 32 * lw + jj) * 8192, NT);
    SB();
    v4u tv[8], to[8];
#pragma unroll
    for (int i = 0; i < 8; ++i) { tv[i] = tcol(wv, i); asm volatile("" : "+v"(tv[i])); }
    lds_wait_slow(ctl + C_OL, 4);
#pragma unroll
    for (int jj = 0; jj < 8; ++jj) wo[jj] = BLD(ro, g * 8 * 1024 + r * 16, (h * 128 + 32 * lw + jj) * 1024 + cq * 256, NT);
    SB();
    lds_wait_slow(ctl + C_LAT, 4);
    {
      const v4u lf = *(const LAS v4u*)(L + L_QA + (r >> 2) * 256 + lw * 64 + g * 16);
      f32x4 ca[8];
#pragma unroll
      for (int i = 0; i < 8; ++i) ca[i] = mfma16(lf, tv[i], f32x4{0.f, 0.f, 0.f, 0.f});
      *(LAS f32x4*)(L + L_RED + (lw * 4 + g) * 512 + r * 32) = f32x4{ca[0][0], ca[1][0], ca[2][0], ca[3][0]};
      *(LAS f32x4*)(L + L_RED + (lw * 4 + g) * 512 + r * 32 + 16) = f32x4{ca[4][0], ca[5][0], ca[6][0], ca[7][0]};
      lds_signal(ctl + C_CTX);
      lds_wait(ctl + C_CTX, 4);
      f32x2 s = *(const LAS f32x2*)(L + L_RED + lw * 512 + lane * 8);
#pragma unroll
      for (int k = 1; k < 4; ++k) s += *(const LAS f32x2*)(L + L_RED + (k * 4 + lw) * 512 + lane * 8);
      BST32(pk_ns(s[0], s[1]), ws, WS_X1 + blk1 * 2048 + lw * 256 + lane * 4);
    }
#pragma unroll
    for (int i = 0; i < 8; ++i) { to[i] = tcol(wo, i); asm volatile("" : "+v"(to[i])); }
    {
      v4u cf;
      const unsigned xb = WS_X1 + (p1 * 128 + h * 4 + (r & 3)) * 2048 + (r >> 2) * 256 + lw * 64 + g * 16;
      for (unsigned spins = 0; spins < SPIN_CAP; ++spins) {
        CFENCE();
        cf = BLD(ws, xb, 0, SC1);
        if (__all(cf.x != SENT && cf.y != SENT && cf.z != SENT && cf.w != SENT)) break;
        __builtin_amdgcn_s_sleep(3);
      }
      float o[8];
#pragma unroll
      for (int i = 0; i < 8; ++i) {
        const f32x4 a = mfma16(cf, to[i], f32x4{0.f, 0.f, 0.f, 0.f});
        o[i] = (a[0] + a[1]) + (a[2] + a[3]);
      }
      *(LAS f32x4*)(L + L_RED3 + (lw * 4 + g) * 512 + r * 32) = f32x4{o[0], o[1], o[2], o[3]};
      *(LAS f32x4*)(L + L_RED3 + (lw * 4 + g) * 512 + r * 32 + 16) = f32x4{o[4], o[5], o[6], o[7]};
      lds_signal(ctl + C_O3);
      lds_wait(ctl + C_O3, 4);
      f32x2 s = *(const LAS f32x2*)(L + L_RED3 + lw * 512 + lane * 8);
#pragma unroll
      for (int k = 1; k < 4; ++k) s += *(const LAS f32x2*)(L + L_RED3 + (k * 4 + lw) * 512 + lane * 8);
      BST32(pk_ns(s[0], s[1]), ws, WS_X1 + blk1 * 2048 + 1024 + lw * 256 + lane * 4);
    }
    lds_signal(ctl + C_WST);
  }
}

extern "C" void kernel_launch(void* const* d_in, const int* in_sizes, int n_in, void* d_out, int out_size, void* d_ws,
                              size_t ws_size, hipStream_t stream) {
  Params P;
  for (int p = 0; p < 2; ++p) {
    P.x[p] = d_in[6 * p + 0]; P.ckv[p] = d_in[6 * p + 1]; P.wq[p] = d_in[6 * p + 2];
    P.wuk[p] = d_in[6 * p + 3]; P.wuv[p] = d_in[6 * p + 4]; P.wo[p] = d_in[6 * p + 5];
  }
  P.out = d_out; P.ws = d_ws;
  (void)hipFuncSetAttribute((const void*)mla_fused, hipFuncAttributeMaxDynamicSharedMemorySize, (int)L_END);
  mla_fused<<<256, 512, L_END, stream>>>(P);
}
```
